```python
import torch

def _softmax(x, axis=-1):
    return torch.softmax(x, dim=axis)
def _gelu(x):
    return torch.nn.functional.gelu(x, approximate="tanh")
def _silu(x):
    return torch.nn.functional.silu(x)
def _sigmoid(x):
    return torch.sigmoid(x)
def _layernorm(x, axis=-1, eps=1e-5):
    m = x.mean(dim=axis, keepdim=True)
    v = x.var(dim=axis, keepdim=True, unbiased=False)
    return (x - m) / torch.sqrt(v + eps)
def _scatter_add(tgt, idx, src, axis=0):
    return tgt.clone().index_add_(axis, idx, src)
def _segment_sum(src, seg, n):
    out = torch.zeros((n,) + src.shape[1:], dtype=src.dtype)
    return out.index_add_(0, seg, src)
def _pad(x, pw):
    flat = tuple(v for pair in reversed(pw) for v in pair)
    return torch.nn.functional.pad(x, flat)

kernel_name = 'concat_heads__mha_block_cached__gqa_block_cached__near_ridge'

def setup_inputs():

    _w = (lambda a: (lambda t: t.to(torch.bfloat16) if t.is_floating_point() else t)(torch.as_tensor(a)))
    def _A_setup():
        B, Sq, D = 16, 96, 448
        H, Sk, Dh = 16, 96, 256
        return {
            "x": _w((lambda *s: torch.randn(*s, dtype=torch.bfloat16) * 0.8)(B, Sq, D)),
            "k_cache": _w((lambda *s: torch.randn(*s, dtype=torch.bfloat16) * 0.8)(B, H, Sk, Dh)),
            "v_cache": _w((lambda *s: torch.randn(*s, dtype=torch.bfloat16) * 0.05)(B, H, Sk, Dh)),
            "wq": _w((lambda *s: torch.randn(*s, dtype=torch.bfloat16) * 0.8)(D, H * Dh) * (D ** -0.5)),
            "wo": _w((lambda *s: torch.randn(*s, dtype=torch.bfloat16) * 0.05)(H * Dh, D) * ((H * Dh) ** -0.5)),
        }
    _A_in = _A_setup()

    def _B_setup():
        B, Sq, D = 16, 96, 448
        Hq, Hkv, Sk, Dh = 16, 16, 96, 256
        assert Hq % Hkv == 0, (Hq, Hkv)
        return {
            "x": _w((lambda *s: torch.randn(*s, dtype=torch.bfloat16) * 0.8)(B, Sq, D)),
            "k_cache": _w((lambda *s: torch.randn(*s, dtype=torch.bfloat16) * 0.8)(B, Hkv, Sk, Dh)),
            "v_cache": _w((lambda *s: torch.randn(*s, dtype=torch.bfloat16) * 0.05)(B, Hkv, Sk, Dh)),
            "wq": _w((lambda *s: torch.randn(*s, dtype=torch.bfloat16) * 0.8)(D, Hq * Dh) * (D ** -0.5)),
            "wo": _w((lambda *s: torch.randn(*s, dtype=torch.bfloat16) * 0.05)(Hq * Dh, D) * ((Hq * Dh) ** -0.5)),
        }
    _B_in = _B_setup()

    return {
        "A_x": _A_in["x"],
        "A_k_cache": _A_in["k_cache"],
        "A_v_cache": _A_in["v_cache"],
        "A_wq": _A_in["wq"],
        "A_wo": _A_in["wo"],
        "B_x": _B_in["x"],
        "B_k_cache": _B_in["k_cache"],
        "B_v_cache": _B_in["v_cache"],
        "B_wq": _B_in["wq"],
        "B_wo": _B_in["wo"],
    }

def reference(A_x, A_k_cache, A_v_cache, A_wq, A_wo, B_x, B_k_cache, B_v_cache, B_wq, B_wo):
    def _A_ref(x, k_cache, v_cache, wq, wo):
        B, Sq, D = x.shape
        H, Dh = 16, 256
        q = (lambda x, *d: x.permute(*d))((x @ wq).reshape(B, Sq, H, Dh), 0, 2, 1, 3)
        scores = torch.einsum("bhqd,bhkd->bhqk", q, k_cache) * (Dh ** -0.5)
        p = _softmax(scores, axis=-1)
        o = torch.einsum("bhqk,bhkd->bhqd", p, v_cache)
        return (lambda x, *d: x.permute(*d))(o, 0, 2, 1, 3).reshape(B, Sq, H * Dh) @ wo

    def _B_ref(x, k_cache, v_cache, wq, wo):
        B, Sq, D = x.shape
        Hq, Dh = 16, 256
        Hkv = k_cache.shape[1]
        rep = Hq // Hkv
        q = (lambda x, *d: x.permute(*d))((x @ wq).reshape(B, Sq, Hq, Dh), 0, 2, 1, 3)
        k = (lambda x, n, axis: torch.repeat_interleave(x, n, dim=axis))(k_cache, rep, axis=1)
        v = (lambda x, n, axis: torch.repeat_interleave(x, n, dim=axis))(v_cache, rep, axis=1)
        scores = torch.einsum("bhqd,bhkd->bhqk", q, k) * (Dh ** -0.5)
        p = _softmax(scores, axis=-1)
        o = torch.einsum("bhqk,bhkd->bhqd", p, v)
        return (lambda x, *d: x.permute(*d))(o, 0, 2, 1, 3).reshape(B, Sq, Hq * Dh) @ wo

    a = _A_ref(A_x, A_k_cache, A_v_cache, A_wq, A_wo)
    b = _B_ref(B_x, B_k_cache, B_v_cache, B_wq, B_wo)
    return torch.concatenate([a, b], axis=-1)
```

```cpp
#include <hip/hip_runtime.h>
#include <hip/hip_bf16.h>

typedef unsigned short u16;
__device__ __forceinline__ float bf2f(u16 v) { return __uint_as_float(((unsigned)v) << 16); }
__device__ __forceinline__ u16 f2bf(float f) { __bf16 b = (__bf16)f; return __builtin_bit_cast(u16, b); }

__global__ void gemm_naive(const u16* A, const u16* B, u16* C, int M, int N, int K, int ldc) {
  __shared__ float As[16][17], Bs[16][17];
  int tx = threadIdx.x & 15, ty = threadIdx.x >> 4;
  int row = blockIdx.y * 16 + ty, col = blockIdx.x * 16 + tx;
  float acc = 0.f;
  for (int k0 = 0; k0 < K; k0 += 16) {
    As[ty][tx] = bf2f(A[(size_t)row * K + k0 + tx]);
    Bs[ty][tx] = bf2f(B[(size_t)(k0 + ty) * N + col]);
    __syncthreads();
    for (int k = 0; k < 16; ++k) acc += As[ty][k] * Bs[k][tx];
    __syncthreads();
  }
  C[(size_t)row * ldc + col] = f2bf(acc);
}

__global__ void attn_naive(const u16* Q, const u16* Kc, const u16* Vc, u16* O) {
  int q = blockIdx.x, h = blockIdx.y, b = blockIdx.z, t = threadIdx.x;
  __shared__ float qs[256], s[96];
  qs[t] = bf2f(Q[((size_t)(b * 96 + q)) * 4096 + h * 256 + t]);
  __syncthreads();
  const u16* Kp = Kc + ((size_t)(b * 16 + h)) * 96 * 256;
  const u16* Vp = Vc + ((size_t)(b * 16 + h)) * 96 * 256;
  if (t < 96) {
    float a = 0.f;
    for (int d = 0; d < 256; ++d) a += qs[d] * bf2f(Kp[t * 256 + d]);
    s[t] = a * 0.0625f;
  }
  __syncthreads();
  float m = -1e30f;
  for (int k = 0; k < 96; ++k) m = fmaxf(m, s[k]);
  float sum = 0.f;
  for (int k = 0; k < 96; ++k) sum += __expf(s[k] - m);
  float o = 0.f;
  for (int k = 0; k < 96; ++k) o += bf2f(f2bf(__expf(s[k] - m) / sum)) * bf2f(Vp[k * 256 + t]);
  O[((size_t)(b * 96 + q)) * 4096 + h * 256 + t] = f2bf(o);
}

extern "C" void kernel_launch(void* const* d_in, const int* in_sizes, int n_in,
                              void* d_out, int out_size, void* d_ws, size_t ws_size,
                              hipStream_t stream) {
  u16* ws = (u16*)d_ws;
  for (int br = 0; br < 2; ++br) {
    const u16* x = (const u16*)d_in[br * 5 + 0];
    const u16* kc = (const u16*)d_in[br * 5 + 1];
    const u16* vc = (const u16*)d_in[br * 5 + 2];
    const u16* wq = (const u16*)d_in[br * 5 + 3];
    const u16* wo = (const u16*)d_in[br * 5 + 4];
    u16* Q = ws + (size_t)br * 2 * 1536 * 4096;
    u16* O = Q + (size_t)1536 * 4096;
    gemm_naive<<<dim3(4096 / 16, 1536 / 16), 256, 0, stream>>>(x, wq, Q, 1536, 4096, 448, 4096);
    attn_naive<<<dim3(96, 16, 16), 256, 0, stream>>>(Q, kc, vc, O);
    gemm_naive<<<dim3(448 / 16, 1536 / 16), 256, 0, stream>>>(O, wo, (u16*)d_out + br * 448, 1536, 448, 4096, 896);
  }
}
```

```cpp
#include <hip/hip_runtime.h>
#include <hip/hip_bf16.h>

typedef unsigned short u16;
__device__ __forceinline__ float bf2f(u16 v) { return __uint_as_float(((unsigned)v) << 16); }
__device__ __forceinline__ u16 f2bf(float f) { __bf16 b = (__bf16)f; return __builtin_bit_cast(u16, b); }

__global__ void gemm_naive(const u16* A, const u16* B, u16* C, int M, int N, int K, int ldc) {
  __shared__ float As[16][17], Bs[16][17];
  int tx = threadIdx.x & 15, ty = threadIdx.x >> 4;
  int row = blockIdx.y * 16 + ty, col = blockIdx.x * 16 + tx;
  float acc = 0.f;
  for (int k0 = 0; k0 < K; k0 += 16) {
    As[ty][tx] = bf2f(A[(size_t)row * K + k0 + tx]);
    Bs[ty][tx] = bf2f(B[(size_t)(k0 + ty) * N + col]);
    __syncthreads();
    for (int k = 0; k < 16; ++k) acc += As[ty][k] * Bs[k][tx];
    __syncthreads();
  }
  C[(size_t)row * ldc + col] = f2bf(acc);
}

__global__ void attn_naive(const u16* Q, const u16* Kc, const u16* Vc, u16* O) {
  int q = blockIdx.x, h = blockIdx.y, b = blockIdx.z, t = threadIdx.x;
  __shared__ float qs[256], s[96];
  qs[t] = bf2f(Q[((size_t)(b * 96 + q)) * 4096 + h * 256 + t]);
  __syncthreads();
  const u16* Kp = Kc + ((size_t)(b * 16 + h)) * 96 * 256;
  const u16* Vp = Vc + ((size_t)(b * 16 + h)) * 96 * 256;
  if (t < 96) {
    float a = 0.f;
    for (int d = 0; d < 256; ++d) a += qs[d] * bf2f(Kp[t * 256 + d]);
    s[t] = a * 0.0625f;
  }
  __syncthreads();
  float m = -1e30f;
  for (int k = 0; k < 96; ++k) m = fmaxf(m, s[k]);
  float sum = 0.f;
  for (int k = 0; k < 96; ++k) sum += __expf(s[k] - m);
  float o = 0.f;
  for (int k = 0; k < 96; ++k) o += bf2f(f2bf(__expf(s[k] - m) / sum)) * bf2f(Vp[k * 256 + t]);
  O[((size_t)(b * 96 + q)) * 4096 + h * 256 + t] = f2bf(o);
}


typedef unsigned u32x4 __attribute__((ext_vector_type(4)));
typedef unsigned long long u64;
template <int VAR>
__global__ __launch_bounds__(256) void probe(const char* kA, const char* vA, const char* kB, const char* vB,
                                             const char* wqA, const char* woA, const char* wqB, const char* woB, char* ws) {
  extern __shared__ __attribute__((aligned(16))) char lds[];
  int wg = blockIdx.x, w = __builtin_amdgcn_readfirstlane(threadIdx.x >> 6), lane = threadIdx.x & 63;
  u64 t0 = __builtin_amdgcn_s_memrealtime();
  unsigned ldsb = (unsigned)(size_t)lds + w * 32768;
  int xcd = wg & 7, idx = wg >> 3;
  int br = xcd >> 2, hp = (xcd & 3) * 2 + (idx & 1), b = idx >> 1;
#define DMA(p, i) do { unsigned dst = ldsb + ((i) & 31) * 1024; \
    if (VAR & 1) asm volatile("s_mov_b32 m0, %1\n\ts_nop 0\n\tglobal_load_lds_dwordx4 %0, off nt\n\ts_waitcnt vmcnt(24)" :: "v"(p), "s"(dst) : "memory"); \
    else asm volatile("s_mov_b32 m0, %1\n\ts_nop 0\n\tglobal_load_lds_dwordx4 %0, off\n\ts_waitcnt vmcnt(24)" :: "v"(p), "s"(dst) : "memory"); } while (0)
  if ((VAR >> 1) == 0) {
    const char* kp = (br ? kB : kA) + (size_t)(b * 16 + hp * 2) * 49152 + w * 1024 + lane * 16;
    const char* vp = (br ? vB : vA) + (size_t)(b * 16 + hp * 2) * 49152 + w * 1024 + lane * 16;
    for (int i = 0; i < 24; ++i) { DMA(kp, i); kp += 4096; }
    for (int i = 0; i < 24; ++i) { DMA(vp, i); vp += 4096; }
  } else if ((VAR >> 1) == 1) {
    const char* qp = (br ? wqB : wqA) + hp * 1024 + (size_t)w * 8192 + lane * 16;
    const char* op = (br ? woB : woA) + (size_t)hp * 458752 + w * 1024 + lane * 16;
    for (int i = 0; i < 112; ++i) { DMA(qp, i); qp += 4 * 8192; }
    for (int i = 0; i < 112; ++i) { DMA(op, i); op += 4096; }
  } else {
    char* p = ws + (64 << 20) + (size_t)wg * 86016 + w * 1024 + lane * 16;
    u32x4 v = {(unsigned)wg, (unsigned)lane, 3u, 4u};
    for (int i = 0; i < 21; ++i) {
      if (VAR & 1) asm volatile("global_store_dwordx4 %0, %1, off sc1\n\ts_nop 1" :: "v"(p), "v"(v) : "memory");
      else asm volatile("global_store_dwordx4 %0, %1, off sc1 nt\n\ts_nop 1" :: "v"(p), "v"(v) : "memory");
      p += 4096;
    }
  }
  asm volatile("s_waitcnt vmcnt(0)" ::: "memory");
  __syncthreads();
  u64 t1 = __builtin_amdgcn_s_memrealtime();
  u64 until = t1 + (t1 - t0) * 100;
  while (__builtin_amdgcn_s_memrealtime() < until) __builtin_amdgcn_s_sleep(8);
}

extern "C" void kernel_launch(void* const* d_in, const int* in_sizes, int n_in,
                              void* d_out, int out_size, void* d_ws, size_t ws_size,
                              hipStream_t stream) {
  u16* ws = (u16*)d_ws;
  hipFuncSetAttribute((const void*)probe<5>, hipFuncAttributeMaxDynamicSharedMemorySize, 131072);
  probe<5><<<256, 256, 131072, stream>>>((const char*)d_in[1], (const char*)d_in[2], (const char*)d_in[6], (const char*)d_in[7],
      (const char*)d_in[3], (const char*)d_in[4], (const char*)d_in[8], (const char*)d_in[9], (char*)d_ws);
  for (int br = 0; br < 2; ++br) {
    const u16* x = (const u16*)d_in[br * 5 + 0];
    const u16* kc = (const u16*)d_in[br * 5 + 1];
    const u16* vc = (const u16*)d_in[br * 5 + 2];
    const u16* wq = (const u16*)d_in[br * 5 + 3];
    const u16* wo = (const u16*)d_in[br * 5 + 4];
    u16* Q = ws + (size_t)br * 2 * 1536 * 4096;
    u16* O = Q + (size_t)1536 * 4096;
    gemm_naive<<<dim3(4096 / 16, 1536 / 16), 256, 0, stream>>>(x, wq, Q, 1536, 4096, 448, 4096);
    attn_naive<<<dim3(96, 16, 16), 256, 0, stream>>>(Q, kc, vc, O);
    gemm_naive<<<dim3(448 / 16, 1536 / 16), 256, 0, stream>>>(O, wo, (u16*)d_out + br * 448, 1536, 448, 4096, 896);
  }
}
```

```cpp
#include <hip/hip_runtime.h>
#include <hip/hip_bf16.h>

typedef unsigned short u16;
__device__ __forceinline__ float bf2f(u16 v) { return __uint_as_float(((unsigned)v) << 16); }
__device__ __forceinline__ u16 f2bf(float f) { __bf16 b = (__bf16)f; return __builtin_bit_cast(u16, b); }

__global__ void gemm_naive(const u16* A, const u16* B, u16* C, int M, int N, int K, int ldc) {
  __shared__ float As[16][17], Bs[16][17];
  int tx = threadIdx.x & 15, ty = threadIdx.x >> 4;
  int row = blockIdx.y * 16 + ty, col = blockIdx.x * 16 + tx;
  float acc = 0.f;
  for (int k0 = 0; k0 < K; k0 += 16) {
    As[ty][tx] = bf2f(A[(size_t)row * K + k0 + tx]);
    Bs[ty][tx] = bf2f(B[(size_t)(k0 + ty) * N + col]);
    __syncthreads();
    for (int k = 0; k < 16; ++k) acc += As[ty][k] * Bs[k][tx];
    __syncthreads();
  }
  C[(size_t)row * ldc + col] = f2bf(acc);
}

__global__ void attn_naive(const u16* Q, const u16* Kc, const u16* Vc, u16* O) {
  int q = blockIdx.x, h = blockIdx.y, b = blockIdx.z, t = threadIdx.x;
  __shared__ float qs[256], s[96];
  qs[t] = bf2f(Q[((size_t)(b * 96 + q)) * 4096 + h * 256 + t]);
  __syncthreads();
  const u16* Kp = Kc + ((size_t)(b * 16 + h)) * 96 * 256;
  const u16* Vp = Vc + ((size_t)(b * 16 + h)) * 96 * 256;
  if (t < 96) {
    float a = 0.f;
    for (int d = 0; d < 256; ++d) a += qs[d] * bf2f(Kp[t * 256 + d]);
    s[t] = a * 0.0625f;
  }
  __syncthreads();
  float m = -1e30f;
  for (int k = 0; k < 96; ++k) m = fmaxf(m, s[k]);
  float sum = 0.f;
  for (int k = 0; k < 96; ++k) sum += __expf(s[k] - m);
  float o = 0.f;
  for (int k = 0; k < 96; ++k) o += bf2f(f2bf(__expf(s[k] - m) / sum)) * bf2f(Vp[k * 256 + t]);
  O[((size_t)(b * 96 + q)) * 4096 + h * 256 + t] = f2bf(o);
}


typedef unsigned long long u64;
__global__ __launch_bounds__(256) void probe(const char* xA) {
  u64 t0, t1;
  asm volatile("s_memrealtime %0\n\ts_waitcnt lgkmcnt(0)\n\ts_memrealtime %1\n\ts_waitcnt lgkmcnt(0)" : "=&s"(t0), "=&s"(t1) :: "memory");
  u64 until = t1 + (t1 - t0) * 200;
  while (__builtin_amdgcn_s_memrealtime() < until) __builtin_amdgcn_s_sleep(8);
}

extern "C" void kernel_launch(void* const* d_in, const int* in_sizes, int n_in,
                              void* d_out, int out_size, void* d_ws, size_t ws_size,
                              hipStream_t stream) {
  u16* ws = (u16*)d_ws;
  probe<<<256, 256, 0, stream>>>((const char*)d_in[0]);
  for (int br = 0; br < 2; ++br) {
    const u16* x = (const u16*)d_in[br * 5 + 0];
    const u16* kc = (const u16*)d_in[br * 5 + 1];
    const u16* vc = (const u16*)d_in[br * 5 + 2];
    const u16* wq = (const u16*)d_in[br * 5 + 3];
    const u16* wo = (const u16*)d_in[br * 5 + 4];
    u16* Q = ws + (size_t)br * 2 * 1536 * 4096;
    u16* O = Q + (size_t)1536 * 4096;
    gemm_naive<<<dim3(4096 / 16, 1536 / 16), 256, 0, stream>>>(x, wq, Q, 1536, 4096, 448, 4096);
    attn_naive<<<dim3(96, 16, 16), 256, 0, stream>>>(Q, kc, vc, O);
    gemm_naive<<<dim3(448 / 16, 1536 / 16), 256, 0, stream>>>(O, wo, (u16*)d_out + br * 448, 1536, 448, 4096, 896);
  }
}
```

```cpp
#include <hip/hip_runtime.h>

__global__ __launch_bounds__(256, 1) void fused_attn(const void* ax, const void* ak, const void* av, const void* awq, const void* awo,
                                                     void* apad, void* aout, void* aws,
                                                     const void* bx, const void* bk, const void* bv, const void* bwq, const void* bwo,
                                                     void* bpad, void* bout, void* bws) {
  __shared__ __attribute__((aligned(16))) char lds[163840];
  auto ka = __builtin_amdgcn_kernarg_segment_ptr();
  unsigned tid = threadIdx.x;
  asm volatile(
      "s_lshl_b32 s3, s2, 4\n"
      "s_and_b32 s3, s3, 64\n"
      "s_load_dwordx16 s[36:51], s[0:1], s3\n"
      "s_getpc_b64 s[74:75]\n"
      "s_bfe_u32 s71, s2, 0x30003\n"
      "s_mul_i32 s71, s71, 0x24\n"
      "v_lshrrev_b32 v1, 2, v0\n"
      "v_min_u32 v1, 0x24, v1\n"
      "v_add_u32 v1, s71, v1\n"
      "v_min_u32 v1, 0x11a, v1\n"
      "v_lshlrev_b32 v1, 7, v1\n"
      "global_load_dword v2, v1, s[74:75]\n"
      "v_readfirstlane_b32 s76, v0\n"
      "v_and_b32 v10, 63, v0\n"
      "v_lshrrev_b32 v11, 4, v10\n"
      "v_lshrrev_b32 v15, 3, v10\n"
      "v_and_b32 v16, 7, v10\n"
      "v_lshlrev_b32 v18, 1, v11\n"
      "s_lshr_b32 s76, s76, 6\n"
      "s_and_b32 s77, s76, 1\n"
      "s_lshr_b32 s78, s76, 1\n"
      "s_and_b32 s72, s2, 3\n"
      "s_lshl_b32 s72, s72, 1\n"
      "s_bfe_u32 s71, s2, 0x10003\n"
      "s_or_b32 s72, s72, s71\n"
      "s_lshr_b32 s73, s2, 4\n"
      "v_xor_b32 v20, v16, v18\n"
      "v_lshlrev_b32 v20, 4, v20\n"
      "v_mul_u32_u24 v21, 896, v15\n"
      "v_add_u32 v21, v21, v20\n"
      "s_mul_i32 s79, s76, 21504\n"
      "s_add_u32 s79, s79, 4096\n"
      "v_add_u32 v220, s79, v21\n"
      "v_add_u32 v221, 6144, v220\n"
      "v_add_u32 v222, 12288, v220\n"
      "v_and_b32 v22, 2, v18\n"
      "v_xor_b32 v22, v22, v16\n"
      "v_lshlrev_b32 v22, 4, v22\n"
      "v_lshl_add_u32 v22, v15, 13, v22\n"
      "s_lshl_b32 s79, s77, 18\n"
      "s_lshl_b32 s3, s78, 9\n"
      "s_add_u32 s79, s79, s3\n"
      "s_add_u32 s79, s79, 4096\n"
      "v_add_u32 v216, s79, v22\n"
      "v_xor_b32 v23, 64, v22\n"
      "v_add_u32 v23, s79, v23\n"
      "v_add_u32 v217, 64512, v23\n"
      "v_add_u32 v218, 129024, v216\n"
      "v_add_u32 v219, 193536, v23\n"
      "s_lshl_b32 s28, s76, 12\n"
      "s_mul_i32 s29, s76, 3072\n"
      "s_mul_i32 s30, s76, 6144\n"
      "s_mul_i32 s71, s71, 3\n"
      "s_and_b32 s31, s73, 3\n"
      "s_mul_i32 s31, s31, 2\n"
      "s_add_u32 s71, s71, s31\n"
      "s_and_b32 s31, s2, 3\n"
      "s_mul_i32 s31, s31, 0\n"
      "s_add_u32 s71, s71, s31\n"
      "s_mul_i32 s79, s71, 43\n"
      "s_lshr_b32 s79, s79, 8\n"
      "s_lshr_b32 s31, s79, 1\n"
      "s_lshl_b32 s74, s79, 1\n"
      "s_add_u32 s74, s74, s31\n"
      "s_mul_i32 s79, s79, 6\n"
      "s_sub_u32 s79, s71, s79\n"
      "s_add_u32 s74, s74, s79\n"
      "s_lshr_b32 s75, s73, 2\n"
      "s_mov_b32 s59, 0\n"
      "s_mov_b32 s52, 0\n"
      "s_add_u32 s79, s79, 1\n"
      "s_cmp_eq_u32 s79, 7\n"
      "s_cselect_b32 s79, 1, s79\n"
      "s_lshl_b32 s60, s79, 7\n"
      "s_lshl_b32 s53, s79, 19\n"
      "s_add_u32 s79, s79, 1\n"
      "s_cmp_eq_u32 s79, 7\n"
      "s_cselect_b32 s79, 1, s79\n"
      "s_lshl_b32 s61, s79, 7\n"
      "s_lshl_b32 s54, s79, 19\n"
      "s_add_u32 s79, s79, 1\n"
      "s_cmp_eq_u32 s79, 7\n"
      "s_cselect_b32 s79, 1, s79\n"
      "s_lshl_b32 s62, s79, 7\n"
      "s_lshl_b32 s55, s79, 19\n"
      "s_add_u32 s79, s79, 1\n"
      "s_cmp_eq_u32 s79, 7\n"
      "s_cselect_b32 s79, 1, s79\n"
      "s_lshl_b32 s63, s79, 7\n"
      "s_lshl_b32 s56, s79, 19\n"
      "s_add_u32 s79, s79, 1\n"
      "s_cmp_eq_u32 s79, 7\n"
      "s_cselect_b32 s79, 1, s79\n"
      "s_lshl_b32 s64, s79, 7\n"
      "s_lshl_b32 s57, s79, 19\n"
      "s_add_u32 s79, s79, 1\n"
      "s_cmp_eq_u32 s79, 7\n"
      "s_cselect_b32 s79, 1, s79\n"
      "s_lshl_b32 s65, s79, 7\n"
      "s_lshl_b32 s58, s79, 19\n"
      "s_add_u32 s71, s75, 0\n"
      "s_and_b32 s71, s71, 3\n"
      "s_lshl_b32 s18, s71, 7\n"
      "s_mul_i32 s22, s71, 57344\n"
      "s_add_u32 s71, s75, 1\n"
      "s_and_b32 s71, s71, 3\n"
      "s_lshl_b32 s19, s71, 7\n"
      "s_mul_i32 s23, s71, 57344\n"
      "s_add_u32 s71, s75, 2\n"
      "s_and_b32 s71, s71, 3\n"
      "s_lshl_b32 s20, s71, 7\n"
      "s_mul_i32 s34, s71, 57344\n"
      "s_add_u32 s71, s75, 3\n"
      "s_and_b32 s71, s71, 3\n"
      "s_lshl_b32 s21, s71, 7\n"
      "s_mul_i32 s35, s71, 57344\n"
      "s_mul_i32 s31, s73, 86016\n"
      "s_lshl_b32 s71, s72, 10\n"
      "s_waitcnt lgkmcnt(0)\n"
      "s_add_u32 s6, s36, s31\n"
      "s_addc_u32 s7, s37, 0\n"
      "s_sub_u32 s6, s6, 4096\n"
      "s_subb_u32 s7, s7, 0\n"
      "s_add_u32 s16, s6, s59\n"
      "s_addc_u32 s17, s7, 0\n"
      "s_add_u32 m0, s29, 65536\n"
      "s_nop 0\n"
      "global_load_lds_dwordx4 v220, s[16:17] offset:0\n"
      "global_load_lds_dwordx4 v221, s[16:17] offset:1024\n"
      "global_load_lds_dwordx4 v222, s[16:17] offset:2048\n"
      "s_add_u32 s4, s42, s71\n"
      "s_addc_u32 s5, s43, 0\n"
      "s_sub_u32 s4, s4, 4096\n"
      "s_subb_u32 s5, s5, 0\n"
      "s_add_u32 s3, s52, s18\n"
      "s_add_u32 s14, s4, s3\n"
      "s_addc_u32 s15, s5, 0\n"
      "s_add_u32 m0, s28, 0\n"
      "s_nop 0\n"
      "global_load_lds_dwordx4 v216, s[14:15] offset:0\n"
      "global_load_lds_dwordx4 v217, s[14:15] offset:1024\n"
      "global_load_lds_dwordx4 v218, s[14:15] offset:2048\n"
      "global_load_lds_dwordx4 v219, s[14:15] offset:3072\n"
      "s_add_u32 s3, s52, s19\n"
      "s_add_u32 s14, s4, s3\n"
      "s_addc_u32 s15, s5, 0\n"
      "s_add_u32 m0, s28, 16384\n"
      "s_nop 0\n"
      "global_load_lds_dwordx4 v216, s[14:15] offset:0\n"
      "global_load_lds_dwordx4 v217, s[14:15] offset:1024\n"
      "global_load_lds_dwordx4 v218, s[14:15] offset:2048\n"
      "global_load_lds_dwordx4 v219, s[14:15] offset:3072\n"
      "s_add_u32 s3, s52, s20\n"
      "s_add_u32 s14, s4, s3\n"
      "s_addc_u32 s15, s5, 0\n"
      "s_lshl_b32 s31, s73, 3\n"
      "s_add_u32 s31, s31, s72\n"
      "s_mul_i32 s31, s31, 98304\n"
      "s_add_u32 s8, s38, s31\n"
      "s_addc_u32 s9, s39, 0\n"
      "s_add_u32 s10, s40, s31\n"
      "s_addc_u32 s11, s41, 0\n"
      "s_mul_i32 s31, s72, 458752\n"
      "s_add_u32 s12, s44, s31\n"
      "s_addc_u32 s13, s45, 0\n"
      "s_sub_u32 s12, s12, 4096\n"
      "s_subb_u32 s13, s13, 0\n"
      "s_mov_b32 s24, s50\n"
      "s_and_b32 s25, s51, 0xffff\n"
      "s_mov_b32 s26, 25559040\n"
      "s_mov_b32 s27, 0x20000\n"
      "s_mov_b32 s40, s48\n"
      "s_and_b32 s41, s49, 0xffff\n"
      "s_mov_b32 s42, 2752512\n"
      "s_mov_b32 s43, 0x20000\n"
      "s_add_u32 s46, s72, s73\n"
      "s_and_b32 s46, s46, 7\n"
      "v_sub_u32 v41, s46, v16\n"
      "v_add_u32 v41, 13, v41\n"
      "v_mul_u32_u24 v42, 37, v41\n"
      "v_lshrrev_b32 v42, 8, v42\n"
      "v_mul_u32_u24 v42, 7, v42\n"
      "v_sub_u32 v41, v41, v42\n"
      "s_cmp_eq_u32 s46, 7\n"
      "s_cselect_b32 s31, 0, s46\n"
      "v_mov_b32 v42, s31\n"
      "v_cmp_eq_u32 vcc, 6, v16\n"
      "s_nop 1\n"
      "v_cndmask_b32_e64 v41, v41, 7, vcc\n"
      "v_cmp_eq_u32 vcc, 7, v16\n"
      "s_nop 1\n"
      "v_cndmask_b32 v41, v41, v42, vcc\n"
      "v_subrev_u32 v41, s73, v41\n"
      "v_and_b32 v42, 1, v41\n"
      "v_bfe_u32 v41, v41, 1, 2\n"
      "v_lshl_add_u32 v41, v42, 3, v41\n"
      "s_and_b32 s31, s2, 0xf4\n"
      "v_add_u32 v41, s31, v41\n"
      "v_lshl_add_u32 v42, v41, 2, s76\n"
      "v_lshlrev_b32 v42, 8, v42\n"
      "v_add_u32 v255, 25165824, v42\n"
      "buffer_load_dword v254, v255, s[24:27], 0 offen offset:0 sc1\n"
      "v_bfe_u32 v12, v10, 2, 2\n"
      "v_and_b32 v13, 3, v10\n"
      "v_and_b32 v14, 15, v10\n"
      "v_and_b32 v17, 2, v18\n"
      "v_lshrrev_b32 v19, 1, v12\n"
      "v_or_b32 v17, v17, v19\n"
      "v_xor_b32 v24, 0, v17\n"
      "v_lshlrev_b32 v25, 5, v24\n"
      "v_lshl_add_u32 v25, v13, 3, v25\n"
      "v_xor_b32 v24, 1, v17\n"
      "v_lshlrev_b32 v26, 5, v24\n"
      "v_lshl_add_u32 v26, v13, 3, v26\n"
      "v_xor_b32 v24, 2, v17\n"
      "v_lshlrev_b32 v27, 5, v24\n"
      "v_lshl_add_u32 v27, v13, 3, v27\n"
      "v_xor_b32 v24, 3, v17\n"
      "v_lshlrev_b32 v28, 5, v24\n"
      "v_lshl_add_u32 v28, v13, 3, v28\n"
      "v_lshl_add_u32 v29, v11, 3, v12\n"
      "v_lshlrev_b32 v29, 7, v29\n"
      "s_lshl_b32 s79, s78, 13\n"
      "v_add_u32 v29, s79, v29\n"
      "v_lshl_add_u32 v36, v11, 2, v12\n"
      "v_lshlrev_b32 v36, 7, v36\n"
      "s_mul_i32 s71, s78, 12288\n"
      "v_add_u32 v35, s71, v36\n"
      "v_add_u32 v235, v29, v25\n"
      "v_add_u32 v245, v35, v25\n"
      "v_add_u32 v236, v29, v26\n"
      "v_add_u32 v246, v35, v26\n"
      "v_add_u32 v237, v29, v27\n"
      "v_add_u32 v247, v35, v27\n"
      "v_add_u32 v238, v29, v28\n"
      "v_add_u32 v248, v35, v28\n"
      "v_and_b32 v30, 6, v14\n"
      "v_xor_b32 v30, v30, v11\n"
      "v_lshlrev_b32 v30, 4, v30\n"
      "v_lshl_add_u32 v30, v14, 7, v30\n"
      "v_xor_b32 v31, 64, v30\n"
      "s_mul_i32 s79, s77, 6144\n"
      "s_add_u32 s31, s79, 65536\n"
      "v_add_u32 v239, s31, v30\n"
      "v_add_u32 v240, s31, v31\n"
      "v_lshrrev_b32 v32, 1, v14\n"
      "v_lshrrev_b32 v33, 1, v11\n"
      "v_xor_b32 v32, v32, v33\n"
      "v_lshlrev_b32 v32, 4, v32\n"
      "v_lshl_add_u32 v32, v14, 7, v32\n"
      "v_and_b32 v33, 1, v11\n"
      "v_lshl_add_u32 v32, v33, 3, v32\n"
      "s_add_u32 s71, s71, 65536\n"
      "v_add_u32 v241, s71, v32\n"
      "v_xor_b32 v34, 32, v32\n"
      "v_add_u32 v242, s71, v34\n"
      "v_xor_b32 v34, 64, v32\n"
      "v_add_u32 v243, s71, v34\n"
      "v_xor_b32 v34, 96, v32\n"
      "v_add_u32 v244, s71, v34\n"
      "v_lshl_add_u32 v36, v13, 3, v36\n"
      "s_lshl_b32 s71, s78, 1\n"
      "v_xor_b32 v37, s71, v17\n"
      "s_lshl_b32 s31, s78, 13\n"
      "v_add_u32 v36, s31, v36\n"
      "v_lshl_add_u32 v249, v37, 5, v36\n"
      "v_xor_b32 v37, 1, v37\n"
      "v_lshl_add_u32 v250, v37, 5, v36\n"
      "v_lshlrev_b32 v38, 4, v10\n"
      "v_add_u32 v251, s30, v38\n"
      "s_xor_b32 s31, s76, 2\n"
      "s_mul_i32 s31, s31, 6144\n"
      "v_add_u32 v252, s31, v38\n"
      "s_mul_i32 s71, s76, 3328\n"
      "v_add_u32 v253, s71, v38\n"
      "v_lshl_add_u32 v44, v10, 2, s71\n"
      "s_mul_i32 s71, s78, 229376\n"
      "s_mul_i32 s31, s77, 28672\n"
      "s_add_u32 s71, s71, s31\n"
      "s_add_u32 s71, s71, 4096\n"
      "v_add_u32 v231, s71, v21\n"
      "v_add_u32 v232, 6144, v231\n"
      "v_add_u32 v233, 12288, v231\n"
      "v_add_u32 v234, 18432, v231\n"
      "s_mul_i32 s71, s76, 24576\n"
      "v_lshl_add_u32 v39, v15, 9, s71\n"
      "v_add_u32 v227, v39, v20\n"
      "v_add_u32 v228, 3072, v227\n"
      "v_add_u32 v229, 6144, v227\n"
      "v_add_u32 v230, 9216, v227\n"
      "v_xor_b32 v40, v16, v11\n"
      "v_lshl_add_u32 v223, v40, 4, v39\n"
      "v_xor_b32 v40, 4, v40\n"
      "v_lshl_add_u32 v40, v40, 4, v39\n"
      "v_add_u32 v224, 3072, v40\n"
      "v_add_u32 v225, 6144, v223\n"
      "v_add_u32 v226, 9216, v40\n"
      "s_mov_b32 s70, 0x3db8aa3b\n"
      "s_mov_b32 s71, 0x3db8aa3b\n"
      "s_add_u32 s69, s46, 1\n"
      "s_sub_u32 s31, s69, 7\n"
      "s_cmp_ge_u32 s69, 7\n"
      "s_cselect_b32 s69, s31, s69\n"
      "s_mul_i32 s68, s69, 13312\n"
      "s_lshl_b32 s69, s69, 7\n"
      "s_mul_i32 s31, s2, 93312\n"
      "s_add_u32 s68, s68, s31\n"
      "s_add_u32 s79, s31, 93184\n"
      "s_lshl_b32 s66, s2, 2\n"
      "s_add_u32 s66, s66, s76\n"
      "s_lshl_b32 s66, s66, 8\n"
      "s_add_u32 s66, s66, 25165824\n"
      "s_mul_i32 s31, s46, 13312\n"
      "s_mov_b32 s74, 93312\n"
      "v_mul_lo_u32 v43, v41, s74\n"
      "v_add_u32 v43, s31, v43\n"
      "s_cmp_eq_u32 s46, 7\n"
      "s_cselect_b32 s74, 25559040, 0\n"
      "v_max_u32 v43, s74, v43\n"
      "s_mul_i32 s74, s73, 172032\n"
      "s_bfe_u32 s75, s2, 0x10002\n"
      "s_mul_i32 s75, s75, 896\n"
      "s_add_u32 s74, s74, s75\n"
      "s_lshl_b32 s75, s46, 7\n"
      "s_add_u32 s74, s74, s75\n"
      "s_lshl_b32 s75, s78, 6\n"
      "s_add_u32 s74, s74, s75\n"
      "s_mul_i32 s75, s77, 86016\n"
      "s_add_u32 s74, s74, s75\n"
      "v_mul_u32_u24 v45, 1792, v14\n"
      "v_lshl_add_u32 v45, v33, 5, v45\n"
      "v_lshrrev_b32 v46, 5, v10\n"
      "v_lshl_add_u32 v45, v46, 4, v45\n"
      "v_add_u32 v45, s74, v45\n"
      "v_accvgpr_write_b32 a232, v43\n"
      "v_accvgpr_write_b32 a233, v44\n"
      "v_accvgpr_write_b32 a234, v45\n"
      "v_accvgpr_write_b32 a235, v10\n"
      "s_sub_u32 s74, s66, 25165824\n"
      "s_lshr_b32 s74, s74, 1\n"
      "s_add_u32 s74, s74, 25427968\n"
      "buffer_load_dword v198, off, s[24:27], s74 offset:0 sc1\n"
      "s_waitcnt vmcnt(6)\n"
      "s_barrier\n"
      "ds_read_b128 a[0:3], v239 offset:0\n"
      "ds_read_b128 a[4:7], v240 offset:0\n"
      "ds_read_b64_tr_b16 a[168:169], v235 offset:0\n"
      "ds_read_b64_tr_b16 a[170:171], v235 offset:512\n"
      "ds_read_b64_tr_b16 a[172:173], v235 offset:4096\n"
      "ds_read_b64_tr_b16 a[174:175], v235 offset:4608\n"
      "ds_read_b64_tr_b16 a[176:177], v236 offset:0\n"
      "ds_read_b64_tr_b16 a[178:179], v236 offset:512\n"
      "ds_read_b64_tr_b16 a[180:181], v236 offset:4096\n"
      "ds_read_b64_tr_b16 a[182:183], v236 offset:4608\n"
      "ds_read_b64_tr_b16 a[184:185], v237 offset:0\n"
      "ds_read_b64_tr_b16 a[186:187], v237 offset:512\n"
      "ds_read_b64_tr_b16 a[188:189], v237 offset:4096\n"
      "ds_read_b64_tr_b16 a[190:191], v237 offset:4608\n"
      "ds_read_b64_tr_b16 a[192:193], v238 offset:0\n"
      "ds_read_b64_tr_b16 a[194:195], v238 offset:512\n"
      "ds_read_b64_tr_b16 a[196:197], v238 offset:4096\n"
      "ds_read_b64_tr_b16 a[198:199], v238 offset:4608\n"
      "ds_read_b128 a[8:11], v239 offset:2048\n"
      "ds_read_b128 a[12:15], v240 offset:2048\n"
      "ds_read_b128 a[16:19], v239 offset:4096\n"
      "ds_read_b128 a[20:23], v240 offset:4096\n"
      "s_add_u32 m0, s28, 32768\n"
      "s_nop 0\n"
      "global_load_lds_dwordx4 v216, s[14:15] offset:0\n"
      "global_load_lds_dwordx4 v217, s[14:15] offset:1024\n"
      "global_load_lds_dwordx4 v218, s[14:15] offset:2048\n"
      "global_load_lds_dwordx4 v219, s[14:15] offset:3072\n"
      "s_add_u32 s3, s52, s21\n"
      "s_add_u32 s14, s4, s3\n"
      "s_addc_u32 s15, s5, 0\n"
      "s_add_u32 s16, s6, s60\n"
      "s_addc_u32 s17, s7, 0\n"
      "s_add_u32 m0, s29, 77824\n"
      "s_nop 0\n"
      "global_load_lds_dwordx4 v220, s[16:17] offset:0\n"
      "global_load_lds_dwordx4 v221, s[16:17] offset:1024\n"
      "global_load_lds_dwordx4 v222, s[16:17] offset:2048\n"
      "s_waitcnt vmcnt(9) lgkmcnt(0)\n"
      "s_barrier\n"
      ".Lfa_q0:\n"
      "v_mfma_f32_16x16x32_bf16 v[0:3], a[168:171], a[0:3], 0\n"
      "s_add_u32 m0, s28, 49152\n"
      "ds_read_b64_tr_b16 a[200:201], v235 offset:16384\n"
      "v_mfma_f32_16x16x32_bf16 v[0:3], a[172:175], a[4:7], v[0:3]\n"
      "ds_read_b64_tr_b16 a[202:203], v235 offset:16896\n"
      "v_mfma_f32_16x16x32_bf16 v[4:7], a[172:175], a[12:15], 0\n"
      "global_load_lds_dwordx4 v216, s[14:15] offset:0\n"
      "v_mfma_f32_16x16x32_bf16 v[4:7], a[168:171], a[8:11], v[4:7]\n"
      "ds_read_b64_tr_b16 a[204:205], v235 offset:20480\n"
      "ds_read_b64_tr_b16 a[206:207], v235 offset:20992\n"
      "v_mfma_f32_16x16x32_bf16 v[8:11], a[168:171], a[16:19], 0\n"
      "ds_read_b64_tr_b16 a[208:209], v236 offset:16384\n"
      "v_mfma_f32_16x16x32_bf16 v[8:11], a[172:175], a[20:23], v[8:11]\n"
      "global_load_lds_dwordx4 v217, s[14:15] offset:1024\n"
      "v_mfma_f32_16x16x32_bf16 v[12:15], a[176:179], a[0:3], 0\n"
      "ds_read_b64_tr_b16 a[210:211], v236 offset:16896\n"
      "v_mfma_f32_16x16x32_bf16 v[12:15], a[180:183], a[4:7], v[12:15]\n"
      "global_load_lds_dwordx4 v218, s[14:15] offset:2048\n"
      "v_mfma_f32_16x16x32_bf16 v[16:19], a[180:183], a[12:15], 0\n"
      "ds_read_b64_tr_b16 a[212:213], v236 offset:20480\n"
      "v_mfma_f32_16x16x32_bf16 v[16:19], a[176:179], a[8:11], v[16:19]\n"
      "ds_read_b64_tr_b16 a[214:215], v236 offset:20992\n"
      "ds_read_b64_tr_b16 a[216:217], v237 offset:16384\n"
      "v_mfma_f32_16x16x32_bf16 v[20:23], a[176:179], a[16:19], 0\n"
      "global_load_lds_dwordx4 v219, s[14:15] offset:3072\n"
      "v_mfma_f32_16x16x32_bf16 v[20:23], a[180:183], a[20:23], v[20:23]\n"
      "ds_read_b64_tr_b16 a[218:219], v237 offset:16896\n"
      "v_mfma_f32_16x16x32_bf16 v[24:27], a[184:187], a[0:3], 0\n"
      "s_add_u32 s3, s53, s18\n"
      "ds_read_b64_tr_b16 a[220:221], v237 offset:20480\n"
      "v_mfma_f32_16x16x32_bf16 v[24:27], a[188:191], a[4:7], v[24:27]\n"
      "ds_read_b64_tr_b16 a[222:223], v237 offset:20992\n"
      "v_mfma_f32_16x16x32_bf16 v[28:31], a[188:191], a[12:15], 0\n"
      "s_add_u32 s14, s4, s3\n"
      "s_addc_u32 s15, s5, 0\n"
      "v_mfma_f32_16x16x32_bf16 v[28:31], a[184:187], a[8:11], v[28:31]\n"
      "ds_read_b64_tr_b16 a[224:225], v238 offset:16384\n"
      "ds_read_b64_tr_b16 a[226:227], v238 offset:16896\n"
      "v_mfma_f32_16x16x32_bf16 v[32:35], a[184:187], a[16:19], 0\n"
      "s_add_u32 m0, s28, 0\n"
      "ds_read_b64_tr_b16 a[228:229], v238 offset:20480\n"
      "v_mfma_f32_16x16x32_bf16 v[32:35], a[188:191], a[20:23], v[32:35]\n"
      "ds_read_b64_tr_b16 a[230:231], v238 offset:20992\n"
      "v_mfma_f32_16x16x32_bf16 v[36:39], a[192:195], a[0:3], 0\n"
      "global_load_lds_dwordx4 v216, s[14:15] offset:0\n"
      "v_mfma_f32_16x16x32_bf16 v[36:39], a[196:199], a[4:7], v[36:39]\n"
      "v_mfma_f32_16x16x32_bf16 v[40:43], a[196:199], a[12:15], 0\n"
      "global_load_lds_dwordx4 v217, s[14:15] offset:1024\n"
      "v_mfma_f32_16x16x32_bf16 v[40:43], a[192:195], a[8:11], v[40:43]\n"
      "v_mfma_f32_16x16x32_bf16 v[44:47], a[192:195], a[16:19], 0\n"
      "s_waitcnt vmcnt(9) lgkmcnt(0)\n"
      "v_mfma_f32_16x16x32_bf16 v[44:47], a[196:199], a[20:23], v[44:47]\n"
      "s_barrier\n"
      "v_mfma_f32_16x16x32_bf16 v[48:51], a[200:203], a[0:3], 0\n"
      "ds_read_b64_tr_b16 a[168:169], v235 offset:32768\n"
      "v_mfma_f32_16x16x32_bf16 v[48:51], a[204:207], a[4:7], v[48:51]\n"
      "global_load_lds_dwordx4 v218, s[14:15] offset:2048\n"
      "v_mfma_f32_16x16x32_bf16 v[52:55], a[204:207], a[12:15], 0\n"
      "ds_read_b64_tr_b16 a[170:171], v235 offset:33280\n"
      "ds_read_b64_tr_b16 a[172:173], v235 offset:36864\n"
      "v_mfma_f32_16x16x32_bf16 v[52:55], a[200:203], a[8:11], v[52:55]\n"
      "global_load_lds_dwordx4 v219, s[14:15] offset:3072\n"
      "v_mfma_f32_16x16x32_bf16 v[56:59], a[200:203], a[16:19], 0\n"
      "ds_read_b64_tr_b16 a[174:175], v235 offset:37376\n"
      "v_mfma_f32_16x16x32_bf16 v[56:59], a[204:207], a[20:23], v[56:59]\n"
      "s_add_u32 s3, s53, s19\n"
      "ds_read_b64_tr_b16 a[176:177], v236 offset:32768\n"
      "v_mfma_f32_16x16x32_bf16 v[60:63], a[208:211], a[0:3], 0\n"
      "s_add_u32 s14, s4, s3\n"
      "s_addc_u32 s15, s5, 0\n"
      "v_mfma_f32_16x16x32_bf16 v[60:63], a[212:215], a[4:7], v[60:63]\n"
      "ds_read_b64_tr_b16 a[178:179], v236 offset:33280\n"
      "ds_read_b64_tr_b16 a[180:181], v236 offset:36864\n"
      "v_mfma_f32_16x16x32_bf16 v[64:67], a[212:215], a[12:15], 0\n"
      "s_add_u32 m0, s28, 16384\n"
      "ds_read_b64_tr_b16 a[182:183], v236 offset:37376\n"
      "v_mfma_f32_16x16x32_bf16 v[64:67], a[208:211], a[8:11], v[64:67]\n"
      "ds_read_b64_tr_b16 a[184:185], v237 offset:32768\n"
      "v_mfma_f32_16x16x32_bf16 v[68:71], a[208:211], a[16:19], 0\n"
      "global_load_lds_dwordx4 v216, s[14:15] offset:0\n"
      "v_mfma_f32_16x16x32_bf16 v[68:71], a[212:215], a[20:23], v[68:71]\n"
      "ds_read_b64_tr_b16 a[186:187], v237 offset:33280\n"
      "v_mfma_f32_16x16x32_bf16 v[72:75], a[216:219], a[0:3], 0\n"
      "global_load_lds_dwordx4 v217, s[14:15] offset:1024\n"
      "v_mfma_f32_16x16x32_bf16 v[72:75], a[220:223], a[4:7], v[72:75]\n"
      "ds_read_b64_tr_b16 a[188:189], v237 offset:36864\n"
      "ds_read_b64_tr_b16 a[190:191], v237 offset:37376\n"
      "v_mfma_f32_16x16x32_bf16 v[76:79], a[220:223], a[12:15], 0\n"
      "ds_read_b64_tr_b16 a[192:193], v238 offset:32768\n"
      "v_mfma_f32_16x16x32_bf16 v[76:79], a[216:219], a[8:11], v[76:79]\n"
      "global_load_lds_dwordx4 v218, s[14:15] offset:2048\n"
      "v_mfma_f32_16x16x32_bf16 v[80:83], a[216:219], a[16:19], 0\n"
      "ds_read_b64_tr_b16 a[194:195], v238 offset:33280\n"
      "ds_read_b64_tr_b16 a[196:197], v238 offset:36864\n"
      "v_mfma_f32_16x16x32_bf16 v[80:83], a[220:223], a[20:23], v[80:83]\n"
      "global_load_lds_dwordx4 v219, s[14:15] offset:3072\n"
      "v_mfma_f32_16x16x32_bf16 v[84:87], a[224:227], a[0:3], 0\n"
      "ds_read_b64_tr_b16 a[198:199], v238 offset:37376\n"
      "v_mfma_f32_16x16x32_bf16 v[84:87], a[228:231], a[4:7], v[84:87]\n"
      "s_add_u32 s3, s53, s20\n"
      "v_mfma_f32_16x16x32_bf16 v[88:91], a[228:231], a[12:15], 0\n"
      "s_add_u32 s14, s4, s3\n"
      "s_addc_u32 s15, s5, 0\n"
      "v_mfma_f32_16x16x32_bf16 v[88:91], a[224:227], a[8:11], v[88:91]\n"
      "v_mfma_f32_16x16x32_bf16 v[92:95], a[224:227], a[16:19], 0\n"
      "s_waitcnt vmcnt(8) lgkmcnt(0)\n"
      "v_mfma_f32_16x16x32_bf16 v[92:95], a[228:231], a[20:23], v[92:95]\n"
      "s_barrier\n"
      "v_mfma_f32_16x16x32_bf16 v[96:99], a[168:171], a[0:3], 0\n"
      "s_add_u32 m0, s28, 32768\n"
      "ds_read_b64_tr_b16 a[200:201], v235 offset:49152\n"
      "v_mfma_f32_16x16x32_bf16 v[96:99], a[172:175], a[4:7], v[96:99]\n"
      "ds_read_b64_tr_b16 a[202:203], v235 offset:49664\n"
      "v_mfma_f32_16x16x32_bf16 v[100:103], a[172:175], a[12:15], 0\n"
      "ds_read_b64_tr_b16 a[204:205], v235 offset:53248\n"
      "v_mfma_f32_16x16x32_bf16 v[100:103], a[168:171], a[8:11], v[100:103]\n"
      "global_load_lds_dwordx4 v216, s[14:15] offset:0\n"
      "v_mfma_f32_16x16x32_bf16 v[104:107], a[168:171], a[16:19], 0\n"
      "ds_read_b64_tr_b16 a[206:207], v235 offset:53760\n"
      "v_mfma_f32_16x16x32_bf16 v[104:107], a[172:175], a[20:23], v[104:107]\n"
      "ds_read_b64_tr_b16 a[208:209], v236 offset:49152\n"
      "v_mfma_f32_16x16x32_bf16 v[108:111], a[176:179], a[0:3], 0\n"
      "ds_read_b64_tr_b16 a[210:211], v236 offset:49664\n"
      "v_mfma_f32_16x16x32_bf16 v[108:111], a[180:183], a[4:7], v[108:111]\n"
      "global_load_lds_dwordx4 v217, s[14:15] offset:1024\n"
      "v_mfma_f32_16x16x32_bf16 v[112:115], a[180:183], a[12:15], 0\n"
      "ds_read_b64_tr_b16 a[212:213], v236 offset:53248\n"
      "v_mfma_f32_16x16x32_bf16 v[112:115], a[176:179], a[8:11], v[112:115]\n"
      "ds_read_b64_tr_b16 a[214:215], v236 offset:53760\n"
      "ds_read_b64_tr_b16 a[216:217], v237 offset:49152\n"
      "v_mfma_f32_16x16x32_bf16 v[116:119], a[176:179], a[16:19], 0\n"
      "global_load_lds_dwordx4 v218, s[14:15] offset:2048\n"
      "v_mfma_f32_16x16x32_bf16 v[116:119], a[180:183], a[20:23], v[116:119]\n"
      "ds_read_b64_tr_b16 a[218:219], v237 offset:49664\n"
      "v_mfma_f32_16x16x32_bf16 v[120:123], a[184:187], a[0:3], 0\n"
      "ds_read_b64_tr_b16 a[220:221], v237 offset:53248\n"
      "v_mfma_f32_16x16x32_bf16 v[120:123], a[188:191], a[4:7], v[120:123]\n"
      "ds_read_b64_tr_b16 a[222:223], v237 offset:53760\n"
      "v_mfma_f32_16x16x32_bf16 v[124:127], a[188:191], a[12:15], 0\n"
      "global_load_lds_dwordx4 v219, s[14:15] offset:3072\n"
      "v_mfma_f32_16x16x32_bf16 v[124:127], a[184:187], a[8:11], v[124:127]\n"
      "ds_read_b64_tr_b16 a[224:225], v238 offset:49152\n"
      "v_mfma_f32_16x16x32_bf16 v[128:131], a[184:187], a[16:19], 0\n"
      "ds_read_b64_tr_b16 a[226:227], v238 offset:49664\n"
      "v_mfma_f32_16x16x32_bf16 v[128:131], a[188:191], a[20:23], v[128:131]\n"
      "s_add_u32 s3, s53, s21\n"
      "ds_read_b64_tr_b16 a[228:229], v238 offset:53248\n"
      "v_mfma_f32_16x16x32_bf16 v[132:135], a[192:195], a[0:3], 0\n"
      "ds_read_b64_tr_b16 a[230:231], v238 offset:53760\n"
      "v_mfma_f32_16x16x32_bf16 v[132:135], a[196:199], a[4:7], v[132:135]\n"
      "v_mfma_f32_16x16x32_bf16 v[136:139], a[196:199], a[12:15], 0\n"
      "s_add_u32 s14, s4, s3\n"
      "s_addc_u32 s15, s5, 0\n"
      "v_mfma_f32_16x16x32_bf16 v[136:139], a[192:195], a[8:11], v[136:139]\n"
      "v_mfma_f32_16x16x32_bf16 v[140:143], a[192:195], a[16:19], 0\n"
      "s_waitcnt vmcnt(8) lgkmcnt(0)\n"
      "v_mfma_f32_16x16x32_bf16 v[140:143], a[196:199], a[20:23], v[140:143]\n"
      "s_barrier\n"
      "v_mfma_f32_16x16x32_bf16 v[144:147], a[200:203], a[0:3], 0\n"
      "s_add_u32 m0, s28, 49152\n"
      "ds_read_b64_tr_b16 a[168:169], v235 offset:0\n"
      "v_mfma_f32_16x16x32_bf16 v[144:147], a[204:207], a[4:7], v[144:147]\n"
      "global_load_lds_dwordx4 v216, s[14:15] offset:0\n"
      "v_mfma_f32_16x16x32_bf16 v[148:151], a[204:207], a[12:15], 0\n"
      "ds_read_b64_tr_b16 a[170:171], v235 offset:512\n"
      "ds_read_b64_tr_b16 a[172:173], v235 offset:4096\n"
      "v_mfma_f32_16x16x32_bf16 v[148:151], a[200:203], a[8:11], v[148:151]\n"
      "global_load_lds_dwordx4 v217, s[14:15] offset:1024\n"
      "v_mfma_f32_16x16x32_bf16 v[152:155], a[200:203], a[16:19], 0\n"
      "ds_read_b64_tr_b16 a[174:175], v235 offset:4608\n"
      "ds_read_b64_tr_b16 a[176:177], v236 offset:0\n"
      "v_mfma_f32_16x16x32_bf16 v[152:155], a[204:207], a[20:23], v[152:155]\n"
      "global_load_lds_dwordx4 v218, s[14:15] offset:2048\n"
      "v_mfma_f32_16x16x32_bf16 v[156:159], a[208:211], a[0:3], 0\n"
      "ds_read_b64_tr_b16 a[178:179], v236 offset:512\n"
      "ds_read_b64_tr_b16 a[180:181], v236 offset:4096\n"
      "ds_read_b64_tr_b16 a[182:183], v236 offset:4608\n"
      "v_mfma_f32_16x16x32_bf16 v[156:159], a[212:215], a[4:7], v[156:159]\n"
      "global_load_lds_dwordx4 v219, s[14:15] offset:3072\n"
      "v_mfma_f32_16x16x32_bf16 v[160:163], a[212:215], a[12:15], 0\n"
      "ds_read_b64_tr_b16 a[184:185], v237 offset:0\n"
      "ds_read_b64_tr_b16 a[186:187], v237 offset:512\n"
      "v_mfma_f32_16x16x32_bf16 v[160:163], a[208:211], a[8:11], v[160:163]\n"
      "s_add_u32 s3, s54, s18\n"
      "ds_read_b64_tr_b16 a[188:189], v237 offset:4096\n"
      "v_mfma_f32_16x16x32_bf16 v[164:167], a[208:211], a[16:19], 0\n"
      "ds_read_b64_tr_b16 a[190:191], v237 offset:4608\n"
      "ds_read_b64_tr_b16 a[192:193], v238 offset:0\n"
      "v_mfma_f32_16x16x32_bf16 v[164:167], a[212:215], a[20:23], v[164:167]\n"
      "s_add_u32 s14, s4, s3\n"
      "s_addc_u32 s15, s5, 0\n"
      "v_mfma_f32_16x16x32_bf16 v[168:171], a[216:219], a[0:3], 0\n"
      "s_add_u32 s16, s6, s61\n"
      "s_addc_u32 s17, s7, 0\n"
      "v_mfma_f32_16x16x32_bf16 v[168:171], a[220:223], a[4:7], v[168:171]\n"
      "s_add_u32 m0, s29, 90112\n"
      "ds_read_b64_tr_b16 a[194:195], v238 offset:512\n"
      "v_mfma_f32_16x16x32_bf16 v[172:175], a[220:223], a[12:15], 0\n"
      "ds_read_b64_tr_b16 a[196:197], v238 offset:4096\n"
      "ds_read_b64_tr_b16 a[198:199], v238 offset:4608\n"
      "v_mfma_f32_16x16x32_bf16 v[172:175], a[216:219], a[8:11], v[172:175]\n"
      "global_load_lds_dwordx4 v220, s[16:17] offset:0\n"
      "v_mfma_f32_16x16x32_bf16 v[176:179], a[216:219], a[16:19], 0\n"
      "ds_read_b128 a[24:27], v239 offset:12288\n"
      "ds_read_b128 a[28:31], v240 offset:12288\n"
      "v_mfma_f32_16x16x32_bf16 v[176:179], a[220:223], a[20:23], v[176:179]\n"
      "global_load_lds_dwordx4 v221, s[16:17] offset:1024\n"
      "v_mfma_f32_16x16x32_bf16 v[180:183], a[224:227], a[0:3], 0\n"
      "ds_read_b128 a[32:35], v239 offset:14336\n"
      "ds_read_b128 a[36:39], v240 offset:14336\n"
      "v_mfma_f32_16x16x32_bf16 v[180:183], a[228:231], a[4:7], v[180:183]\n"
      "global_load_lds_dwordx4 v222, s[16:17] offset:2048\n"
      "v_mfma_f32_16x16x32_bf16 v[184:187], a[228:231], a[12:15], 0\n"
      "ds_read_b128 a[40:43], v239 offset:16384\n"
      "ds_read_b128 a[44:47], v240 offset:16384\n"
      "v_mfma_f32_16x16x32_bf16 v[184:187], a[224:227], a[8:11], v[184:187]\n"
      "s_add_u32 s16, s6, s62\n"
      "s_addc_u32 s17, s7, 0\n"
      "v_mfma_f32_16x16x32_bf16 v[188:191], a[224:227], a[16:19], 0\n"
      "s_waitcnt vmcnt(11) lgkmcnt(0)\n"
      "v_mfma_f32_16x16x32_bf16 v[188:191], a[228:231], a[20:23], v[188:191]\n"
      "s_barrier\n"
      "v_mfma_f32_16x16x32_bf16 v[0:3], a[168:171], a[24:27], v[0:3]\n"
      "s_add_u32 m0, s28, 0\n"
      "ds_read_b64_tr_b16 a[200:201], v235 offset:16384\n"
      "v_mfma_f32_16x16x32_bf16 v[0:3], a[172:175], a[28:31], v[0:3]\n"
      "ds_read_b64_tr_b16 a[202:203], v235 offset:16896\n"
      "v_mfma_f32_16x16x32_bf16 v[4:7], a[172:175], a[36:39], v[4:7]\n"
      "ds_read_b64_tr_b16 a[204:205], v235 offset:20480\n"
      "v_mfma_f32_16x16x32_bf16 v[4:7], a[168:171], a[32:35], v[4:7]\n"
      "global_load_lds_dwordx4 v216, s[14:15] offset:0\n"
      "v_mfma_f32_16x16x32_bf16 v[8:11], a[168:171], a[40:43], v[8:11]\n"
      "ds_read_b64_tr_b16 a[206:207], v235 offset:20992\n"
      "v_mfma_f32_16x16x32_bf16 v[8:11], a[172:175], a[44:47], v[8:11]\n"
      "ds_read_b64_tr_b16 a[208:209], v236 offset:16384\n"
      "v_mfma_f32_16x16x32_bf16 v[12:15], a[176:179], a[24:27], v[12:15]\n"
      "ds_read_b64_tr_b16 a[210:211], v236 offset:16896\n"
      "v_mfma_f32_16x16x32_bf16 v[12:15], a[180:183], a[28:31], v[12:15]\n"
      "global_load_lds_dwordx4 v217, s[14:15] offset:1024\n"
      "v_mfma_f32_16x16x32_bf16 v[16:19], a[180:183], a[36:39], v[16:19]\n"
      "ds_read_b64_tr_b16 a[212:213], v236 offset:20480\n"
      "v_mfma_f32_16x16x32_bf16 v[16:19], a[176:179], a[32:35], v[16:19]\n"
      "ds_read_b64_tr_b16 a[214:215], v236 offset:20992\n"
      "ds_read_b64_tr_b16 a[216:217], v237 offset:16384\n"
      "v_mfma_f32_16x16x32_bf16 v[20:23], a[176:179], a[40:43], v[20:23]\n"
      "global_load_lds_dwordx4 v218, s[14:15] offset:2048\n"
      "v_mfma_f32_16x16x32_bf16 v[20:23], a[180:183], a[44:47], v[20:23]\n"
      "ds_read_b64_tr_b16 a[218:219], v237 offset:16896\n"
      "v_mfma_f32_16x16x32_bf16 v[24:27], a[184:187], a[24:27], v[24:27]\n"
      "ds_read_b64_tr_b16 a[220:221], v237 offset:20480\n"
      "v_mfma_f32_16x16x32_bf16 v[24:27], a[188:191], a[28:31], v[24:27]\n"
      "ds_read_b64_tr_b16 a[222:223], v237 offset:20992\n"
      "v_mfma_f32_16x16x32_bf16 v[28:31], a[188:191], a[36:39], v[28:31]\n"
      "global_load_lds_dwordx4 v219, s[14:15] offset:3072\n"
      "v_mfma_f32_16x16x32_bf16 v[28:31], a[184:187], a[32:35], v[28:31]\n"
      "ds_read_b64_tr_b16 a[224:225], v238 offset:16384\n"
      "v_mfma_f32_16x16x32_bf16 v[32:35], a[184:187], a[40:43], v[32:35]\n"
      "ds_read_b64_tr_b16 a[226:227], v238 offset:16896\n"
      "v_mfma_f32_16x16x32_bf16 v[32:35], a[188:191], a[44:47], v[32:35]\n"
      "s_add_u32 s3, s54, s19\n"
      "ds_read_b64_tr_b16 a[228:229], v238 offset:20480\n"
      "v_mfma_f32_16x16x32_bf16 v[36:39], a[192:195], a[24:27], v[36:39]\n"
      "ds_read_b64_tr_b16 a[230:231], v238 offset:20992\n"
      "v_mfma_f32_16x16x32_bf16 v[36:39], a[196:199], a[28:31], v[36:39]\n"
      "v_mfma_f32_16x16x32_bf16 v[40:43], a[196:199], a[36:39], v[40:43]\n"
      "s_add_u32 s14, s4, s3\n"
      "s_addc_u32 s15, s5, 0\n"
      "v_mfma_f32_16x16x32_bf16 v[40:43], a[192:195], a[32:35], v[40:43]\n"
      "v_mfma_f32_16x16x32_bf16 v[44:47], a[192:195], a[40:43], v[44:47]\n"
      "s_waitcnt vmcnt(11) lgkmcnt(0)\n"
      "v_mfma_f32_16x16x32_bf16 v[44:47], a[196:199], a[44:47], v[44:47]\n"
      "s_barrier\n"
      "v_mfma_f32_16x16x32_bf16 v[48:51], a[200:203], a[24:27], v[48:51]\n"
      "s_add_u32 m0, s28, 16384\n"
      "ds_read_b64_tr_b16 a[168:169], v235 offset:32768\n"
      "v_mfma_f32_16x16x32_bf16 v[48:51], a[204:207], a[28:31], v[48:51]\n"
      "ds_read_b64_tr_b16 a[170:171], v235 offset:33280\n"
      "ds_read_b64_tr_b16 a[172:173], v235 offset:36864\n"
      "v_mfma_f32_16x16x32_bf16 v[52:55], a[204:207], a[36:39], v[52:55]\n"
      "global_load_lds_dwordx4 v216, s[14:15] offset:0\n"
      "v_mfma_f32_16x16x32_bf16 v[52:55], a[200:203], a[32:35], v[52:55]\n"
      "ds_read_b64_tr_b16 a[174:175], v235 offset:37376\n"
      "v_mfma_f32_16x16x32_bf16 v[56:59], a[200:203], a[40:43], v[56:59]\n"
      "global_load_lds_dwordx4 v217, s[14:15] offset:1024\n"
      "v_mfma_f32_16x16x32_bf16 v[56:59], a[204:207], a[44:47], v[56:59]\n"
      "ds_read_b64_tr_b16 a[176:177], v236 offset:32768\n"
      "ds_read_b64_tr_b16 a[178:179], v236 offset:33280\n"
      "v_mfma_f32_16x16x32_bf16 v[60:63], a[208:211], a[24:27], v[60:63]\n"
      "global_load_lds_dwordx4 v218, s[14:15] offset:2048\n"
      "v_mfma_f32_16x16x32_bf16 v[60:63], a[212:215], a[28:31], v[60:63]\n"
      "ds_read_b64_tr_b16 a[180:181], v236 offset:36864\n"
      "v_mfma_f32_16x16x32_bf16 v[64:67], a[212:215], a[36:39], v[64:67]\n"
      "global_load_lds_dwordx4 v219, s[14:15] offset:3072\n"
      "v_mfma_f32_16x16x32_bf16 v[64:67], a[208:211], a[32:35], v[64:67]\n"
      "s_add_u32 s3, s54, s20\n"
      "ds_read_b64_tr_b16 a[182:183], v236 offset:37376\n"
      "v_mfma_f32_16x16x32_bf16 v[68:71], a[208:211], a[40:43], v[68:71]\n"
      "ds_read_b64_tr_b16 a[184:185], v237 offset:32768\n"
      "ds_read_b64_tr_b16 a[186:187], v237 offset:33280\n"
      "v_mfma_f32_16x16x32_bf16 v[68:71], a[212:215], a[44:47], v[68:71]\n"
      "s_add_u32 s14, s4, s3\n"
      "s_addc_u32 s15, s5, 0\n"
      "v_mfma_f32_16x16x32_bf16 v[72:75], a[216:219], a[24:27], v[72:75]\n"
      "ds_read_b64_tr_b16 a[188:189], v237 offset:36864\n"
      "ds_read_b64_tr_b16 a[190:191], v237 offset:37376\n"
      "v_mfma_f32_16x16x32_bf16 v[72:75], a[220:223], a[28:31], v[72:75]\n"
      "s_add_u32 m0, s29, 102400\n"
      "ds_read_b64_tr_b16 a[192:193], v238 offset:32768\n"
      "v_mfma_f32_16x16x32_bf16 v[76:79], a[220:223], a[36:39], v[76:79]\n"
      "global_load_lds_dwordx4 v220, s[16:17] offset:0\n"
      "v_mfma_f32_16x16x32_bf16 v[76:79], a[216:219], a[32:35], v[76:79]\n"
      "ds_read_b64_tr_b16 a[194:195], v238 offset:33280\n"
      "v_mfma_f32_16x16x32_bf16 v[80:83], a[216:219], a[40:43], v[80:83]\n"
      "global_load_lds_dwordx4 v221, s[16:17] offset:1024\n"
      "v_mfma_f32_16x16x32_bf16 v[80:83], a[220:223], a[44:47], v[80:83]\n"
      "ds_read_b64_tr_b16 a[196:197], v238 offset:36864\n"
      "ds_read_b64_tr_b16 a[198:199], v238 offset:37376\n"
      "v_mfma_f32_16x16x32_bf16 v[84:87], a[224:227], a[24:27], v[84:87]\n"
      "global_load_lds_dwordx4 v222, s[16:17] offset:2048\n"
      "v_mfma_f32_16x16x32_bf16 v[84:87], a[228:231], a[28:31], v[84:87]\n"
      "v_mfma_f32_16x16x32_bf16 v[88:91], a[228:231], a[36:39], v[88:91]\n"
      "s_add_u32 s16, s8, s18\n"
      "s_addc_u32 s17, s9, 0\n"
      "v_mfma_f32_16x16x32_bf16 v[88:91], a[224:227], a[32:35], v[88:91]\n"
      "v_mfma_f32_16x16x32_bf16 v[92:95], a[224:227], a[40:43], v[92:95]\n"
      "s_waitcnt vmcnt(14) lgkmcnt(0)\n"
      "v_mfma_f32_16x16x32_bf16 v[92:95], a[228:231], a[44:47], v[92:95]\n"
      "s_barrier\n"
      "v_mfma_f32_16x16x32_bf16 v[96:99], a[168:171], a[24:27], v[96:99]\n"
      "s_add_u32 m0, s28, 32768\n"
      "ds_read_b64_tr_b16 a[200:201], v235 offset:49152\n"
      "v_mfma_f32_16x16x32_bf16 v[96:99], a[172:175], a[28:31], v[96:99]\n"
      "ds_read_b64_tr_b16 a[202:203], v235 offset:49664\n"
      "v_mfma_f32_16x16x32_bf16 v[100:103], a[172:175], a[36:39], v[100:103]\n"
      "ds_read_b64_tr_b16 a[204:205], v235 offset:53248\n"
      "v_mfma_f32_16x16x32_bf16 v[100:103], a[168:171], a[32:35], v[100:103]\n"
      "global_load_lds_dwordx4 v216, s[14:15] offset:0\n"
      "v_mfma_f32_16x16x32_bf16 v[104:107], a[168:171], a[40:43], v[104:107]\n"
      "ds_read_b64_tr_b16 a[206:207], v235 offset:53760\n"
      "v_mfma_f32_16x16x32_bf16 v[104:107], a[172:175], a[44:47], v[104:107]\n"
      "ds_read_b64_tr_b16 a[208:209], v236 offset:49152\n"
      "v_mfma_f32_16x16x32_bf16 v[108:111], a[176:179], a[24:27], v[108:111]\n"
      "ds_read_b64_tr_b16 a[210:211], v236 offset:49664\n"
      "v_mfma_f32_16x16x32_bf16 v[108:111], a[180:183], a[28:31], v[108:111]\n"
      "global_load_lds_dwordx4 v217, s[14:15] offset:1024\n"
      "v_mfma_f32_16x16x32_bf16 v[112:115], a[180:183], a[36:39], v[112:115]\n"
      "ds_read_b64_tr_b16 a[212:213], v236 offset:53248\n"
      "v_mfma_f32_16x16x32_bf16 v[112:115], a[176:179], a[32:35], v[112:115]\n"
      "ds_read_b64_tr_b16 a[214:215], v236 offset:53760\n"
      "ds_read_b64_tr_b16 a[216:217], v237 offset:49152\n"
      "v_mfma_f32_16x16x32_bf16 v[116:119], a[176:179], a[40:43], v[116:119]\n"
      "global_load_lds_dwordx4 v218, s[14:15] offset:2048\n"
      "v_mfma_f32_16x16x32_bf16 v[116:119], a[180:183], a[44:47], v[116:119]\n"
      "ds_read_b64_tr_b16 a[218:219], v237 offset:49664\n"
      "v_mfma_f32_16x16x32_bf16 v[120:123], a[184:187], a[24:27], v[120:123]\n"
      "ds_read_b64_tr_b16 a[220:221], v237 offset:53248\n"
      "v_mfma_f32_16x16x32_bf16 v[120:123], a[188:191], a[28:31], v[120:123]\n"
      "ds_read_b64_tr_b16 a[222:223], v237 offset:53760\n"
      "v_mfma_f32_16x16x32_bf16 v[124:127], a[188:191], a[36:39], v[124:127]\n"
      "global_load_lds_dwordx4 v219, s[14:15] offset:3072\n"
      "v_mfma_f32_16x16x32_bf16 v[124:127], a[184:187], a[32:35], v[124:127]\n"
      "ds_read_b64_tr_b16 a[224:225], v238 offset:49152\n"
      "v_mfma_f32_16x16x32_bf16 v[128:131], a[184:187], a[40:43], v[128:131]\n"
      "ds_read_b64_tr_b16 a[226:227], v238 offset:49664\n"
      "v_mfma_f32_16x16x32_bf16 v[128:131], a[188:191], a[44:47], v[128:131]\n"
      "s_add_u32 s3, s54, s21\n"
      "ds_read_b64_tr_b16 a[228:229], v238 offset:53248\n"
      "v_mfma_f32_16x16x32_bf16 v[132:135], a[192:195], a[24:27], v[132:135]\n"
      "ds_read_b64_tr_b16 a[230:231], v238 offset:53760\n"
      "v_mfma_f32_16x16x32_bf16 v[132:135], a[196:199], a[28:31], v[132:135]\n"
      "v_mfma_f32_16x16x32_bf16 v[136:139], a[196:199], a[36:39], v[136:139]\n"
      "s_add_u32 s14, s4, s3\n"
      "s_addc_u32 s15, s5, 0\n"
      "v_mfma_f32_16x16x32_bf16 v[136:139], a[192:195], a[32:35], v[136:139]\n"
      "v_mfma_f32_16x16x32_bf16 v[140:143], a[192:195], a[40:43], v[140:143]\n"
      "s_waitcnt vmcnt(11) lgkmcnt(0)\n"
      "v_mfma_f32_16x16x32_bf16 v[140:143], a[196:199], a[44:47], v[140:143]\n"
      "s_barrier\n"
      "v_mfma_f32_16x16x32_bf16 v[144:147], a[200:203], a[24:27], v[144:147]\n"
      "s_add_u32 m0, s28, 49152\n"
      "ds_read_b64_tr_b16 a[168:169], v235 offset:0\n"
      "v_mfma_f32_16x16x32_bf16 v[144:147], a[204:207], a[28:31], v[144:147]\n"
      "global_load_lds_dwordx4 v216, s[14:15] offset:0\n"
      "v_mfma_f32_16x16x32_bf16 v[148:151], a[204:207], a[36:39], v[148:151]\n"
      "global_load_lds_dwordx4 v217, s[14:15] offset:1024\n"
      "v_mfma_f32_16x16x32_bf16 v[148:151], a[200:203], a[32:35], v[148:151]\n"
      "ds_read_b64_tr_b16 a[170:171], v235 offset:512\n"
      "ds_read_b64_tr_b16 a[172:173], v235 offset:4096\n"
      "v_mfma_f32_16x16x32_bf16 v[152:155], a[200:203], a[40:43], v[152:155]\n"
      "global_load_lds_dwordx4 v218, s[14:15] offset:2048\n"
      "v_mfma_f32_16x16x32_bf16 v[152:155], a[204:207], a[44:47], v[152:155]\n"
      "global_load_lds_dwordx4 v219, s[14:15] offset:3072\n"
      "v_mfma_f32_16x16x32_bf16 v[156:159], a[208:211], a[24:27], v[156:159]\n"
      "s_add_u32 s3, s55, s18\n"
      "ds_read_b64_tr_b16 a[174:175], v235 offset:4608\n"
      "v_mfma_f32_16x16x32_bf16 v[156:159], a[212:215], a[28:31], v[156:159]\n"
      "s_add_u32 s14, s4, s3\n"
      "s_addc_u32 s15, s5, 0\n"
      "ds_read_b64_tr_b16 a[176:177], v236 offset:0\n"
      "ds_read_b64_tr_b16 a[178:179], v236 offset:512\n"
      "v_mfma_f32_16x16x32_bf16 v[160:163], a[212:215], a[36:39], v[160:163]\n"
      "ds_read_b64_tr_b16 a[180:181], v236 offset:4096\n"
      "ds_read_b64_tr_b16 a[182:183], v236 offset:4608\n"
      "ds_read_b64_tr_b16 a[184:185], v237 offset:0\n"
      "ds_read_b64_tr_b16 a[186:187], v237 offset:512\n"
      "v_mfma_f32_16x16x32_bf16 v[160:163], a[208:211], a[32:35], v[160:163]\n"
      "s_add_u32 m0, s30, 65536\n"
      "ds_read_b64_tr_b16 a[188:189], v237 offset:4096\n"
      "v_mfma_f32_16x16x32_bf16 v[164:167], a[208:211], a[40:43], v[164:167]\n"
      "global_load_lds_dwordx4 v223, s[16:17] offset:0 nt\n"
      "v_mfma_f32_16x16x32_bf16 v[164:167], a[212:215], a[44:47], v[164:167]\n"
      "global_load_lds_dwordx4 v224, s[16:17] offset:1024 nt\n"
      "v_mfma_f32_16x16x32_bf16 v[168:171], a[216:219], a[24:27], v[168:171]\n"
      "ds_read_b64_tr_b16 a[190:191], v237 offset:4608\n"
      "ds_read_b64_tr_b16 a[192:193], v238 offset:0\n"
      "ds_read_b64_tr_b16 a[194:195], v238 offset:512\n"
      "v_mfma_f32_16x16x32_bf16 v[168:171], a[220:223], a[28:31], v[168:171]\n"
      "global_load_lds_dwordx4 v225, s[16:17] offset:2048 nt\n"
      "v_mfma_f32_16x16x32_bf16 v[172:175], a[220:223], a[36:39], v[172:175]\n"
      "global_load_lds_dwordx4 v226, s[16:17] offset:3072 nt\n"
      "v_mfma_f32_16x16x32_bf16 v[172:175], a[216:219], a[32:35], v[172:175]\n"
      "ds_read_b64_tr_b16 a[196:197], v238 offset:4096\n"
      "ds_read_b64_tr_b16 a[198:199], v238 offset:4608\n"
      "ds_read_b128 a[48:51], v239 offset:24576\n"
      "v_mfma_f32_16x16x32_bf16 v[176:179], a[216:219], a[40:43], v[176:179]\n"
      "ds_read_b128 a[52:55], v240 offset:24576\n"
      "ds_read_b128 a[56:59], v239 offset:26624\n"
      "v_mfma_f32_16x16x32_bf16 v[176:179], a[220:223], a[44:47], v[176:179]\n"
      "s_add_u32 s16, s16, 16384\n"
      "s_addc_u32 s17, s17, 0\n"
      "s_add_u32 m0, s30, 69632\n"
      "ds_read_b128 a[60:63], v240 offset:26624\n"
      "v_mfma_f32_16x16x32_bf16 v[180:183], a[224:227], a[24:27], v[180:183]\n"
      "global_load_lds_dwordx4 v223, s[16:17] offset:0 nt\n"
      "v_mfma_f32_16x16x32_bf16 v[180:183], a[228:231], a[28:31], v[180:183]\n"
      "global_load_lds_dwordx4 v224, s[16:17] offset:1024 nt\n"
      "v_mfma_f32_16x16x32_bf16 v[184:187], a[228:231], a[36:39], v[184:187]\n"
      "ds_read_b128 a[64:67], v239 offset:28672\n"
      "ds_read_b128 a[68:71], v240 offset:28672\n"
      "v_mfma_f32_16x16x32_bf16 v[184:187], a[224:227], a[32:35], v[184:187]\n"
      "s_add_u32 s16, s6, s63\n"
      "s_addc_u32 s17, s7, 0\n"
      "v_mfma_f32_16x16x32_bf16 v[188:191], a[224:227], a[40:43], v[188:191]\n"
      "s_waitcnt vmcnt(17) lgkmcnt(0)\n"
      "v_mfma_f32_16x16x32_bf16 v[188:191], a[228:231], a[44:47], v[188:191]\n"
      "s_barrier\n"
      "v_mfma_f32_16x16x32_bf16 v[0:3], a[168:171], a[48:51], v[0:3]\n"
      "s_add_u32 m0, s28, 0\n"
      "ds_read_b64_tr_b16 a[200:201], v235 offset:16384\n"
      "v_mfma_f32_16x16x32_bf16 v[0:3], a[172:175], a[52:55], v[0:3]\n"
      "ds_read_b64_tr_b16 a[202:203], v235 offset:16896\n"
      "v_mfma_f32_16x16x32_bf16 v[4:7], a[172:175], a[60:63], v[4:7]\n"
      "ds_read_b64_tr_b16 a[204:205], v235 offset:20480\n"
      "v_mfma_f32_16x16x32_bf16 v[4:7], a[168:171], a[56:59], v[4:7]\n"
      "global_load_lds_dwordx4 v216, s[14:15] offset:0\n"
      "v_mfma_f32_16x16x32_bf16 v[8:11], a[168:171], a[64:67], v[8:11]\n"
      "ds_read_b64_tr_b16 a[206:207], v235 offset:20992\n"
      "v_mfma_f32_16x16x32_bf16 v[8:11], a[172:175], a[68:71], v[8:11]\n"
      "ds_read_b64_tr_b16 a[208:209], v236 offset:16384\n"
      "v_mfma_f32_16x16x32_bf16 v[12:15], a[176:179], a[48:51], v[12:15]\n"
      "ds_read_b64_tr_b16 a[210:211], v236 offset:16896\n"
      "v_mfma_f32_16x16x32_bf16 v[12:15], a[180:183], a[52:55], v[12:15]\n"
      "global_load_lds_dwordx4 v217, s[14:15] offset:1024\n"
      "v_mfma_f32_16x16x32_bf16 v[16:19], a[180:183], a[60:63], v[16:19]\n"
      "ds_read_b64_tr_b16 a[212:213], v236 offset:20480\n"
      "v_mfma_f32_16x16x32_bf16 v[16:19], a[176:179], a[56:59], v[16:19]\n"
      "ds_read_b64_tr_b16 a[214:215], v236 offset:20992\n"
      "ds_read_b64_tr_b16 a[216:217], v237 offset:16384\n"
      "v_mfma_f32_16x16x32_bf16 v[20:23], a[176:179], a[64:67], v[20:23]\n"
      "global_load_lds_dwordx4 v218, s[14:15] offset:2048\n"
      "v_mfma_f32_16x16x32_bf16 v[20:23], a[180:183], a[68:71], v[20:23]\n"
      "ds_read_b64_tr_b16 a[218:219], v237 offset:16896\n"
      "v_mfma_f32_16x16x32_bf16 v[24:27], a[184:187], a[48:51], v[24:27]\n"
      "ds_read_b64_tr_b16 a[220:221], v237 offset:20480\n"
      "v_mfma_f32_16x16x32_bf16 v[24:27], a[188:191], a[52:55], v[24:27]\n"
      "ds_read_b64_tr_b16 a[222:223], v237 offset:20992\n"
      "v_mfma_f32_16x16x32_bf16 v[28:31], a[188:191], a[60:63], v[28:31]\n"
      "global_load_lds_dwordx4 v219, s[14:15] offset:3072\n"
      "v_mfma_f32_16x16x32_bf16 v[28:31], a[184:187], a[56:59], v[28:31]\n"
      "ds_read_b64_tr_b16 a[224:225], v238 offset:16384\n"
      "v_mfma_f32_16x16x32_bf16 v[32:35], a[184:187], a[64:67], v[32:35]\n"
      "ds_read_b64_tr_b16 a[226:227], v238 offset:16896\n"
      "v_mfma_f32_16x16x32_bf16 v[32:35], a[188:191], a[68:71], v[32:35]\n"
      "s_add_u32 s3, s55, s19\n"
      "ds_read_b64_tr_b16 a[228:229], v238 offset:20480\n"
      "v_mfma_f32_16x16x32_bf16 v[36:39], a[192:195], a[48:51], v[36:39]\n"
      "ds_read_b64_tr_b16 a[230:231], v238 offset:20992\n"
      "v_mfma_f32_16x16x32_bf16 v[36:39], a[196:199], a[52:55], v[36:39]\n"
      "v_mfma_f32_16x16x32_bf16 v[40:43], a[196:199], a[60:63], v[40:43]\n"
      "s_add_u32 s14, s4, s3\n"
      "s_addc_u32 s15, s5, 0\n"
      "v_mfma_f32_16x16x32_bf16 v[40:43], a[192:195], a[56:59], v[40:43]\n"
      "v_mfma_f32_16x16x32_bf16 v[44:47], a[192:195], a[64:67], v[44:47]\n"
      "s_waitcnt vmcnt(14) lgkmcnt(0)\n"
      "v_mfma_f32_16x16x32_bf16 v[44:47], a[196:199], a[68:71], v[44:47]\n"
      "s_barrier\n"
      "v_mfma_f32_16x16x32_bf16 v[48:51], a[200:203], a[48:51], v[48:51]\n"
      "ds_read_b64_tr_b16 a[168:169], v235 offset:32768\n"
      "ds_read_b64_tr_b16 a[170:171], v235 offset:33280\n"
      "v_mfma_f32_16x16x32_bf16 v[48:51], a[204:207], a[52:55], v[48:51]\n"
      "s_add_u32 m0, s28, 16384\n"
      "ds_read_b64_tr_b16 a[172:173], v235 offset:36864\n"
      "v_mfma_f32_16x16x32_bf16 v[52:55], a[204:207], a[60:63], v[52:55]\n"
      "global_load_lds_dwordx4 v216, s[14:15] offset:0\n"
      "v_mfma_f32_16x16x32_bf16 v[52:55], a[200:203], a[56:59], v[52:55]\n"
      "ds_read_b64_tr_b16 a[174:175], v235 offset:37376\n"
      "ds_read_b64_tr_b16 a[176:177], v236 offset:32768\n"
      "v_mfma_f32_16x16x32_bf16 v[56:59], a[200:203], a[64:67], v[56:59]\n"
      "global_load_lds_dwordx4 v217, s[14:15] offset:1024\n"
      "v_mfma_f32_16x16x32_bf16 v[56:59], a[204:207], a[68:71], v[56:59]\n"
      "ds_read_b64_tr_b16 a[178:179], v236 offset:33280\n"
      "ds_read_b64_tr_b16 a[180:181], v236 offset:36864\n"
      "v_mfma_f32_16x16x32_bf16 v[60:63], a[208:211], a[48:51], v[60:63]\n"
      "global_load_lds_dwordx4 v218, s[14:15] offset:2048\n"
      "v_mfma_f32_16x16x32_bf16 v[60:63], a[212:215], a[52:55], v[60:63]\n"
      "ds_read_b64_tr_b16 a[182:183], v236 offset:37376\n"
      "ds_read_b64_tr_b16 a[184:185], v237 offset:32768\n"
      "v_mfma_f32_16x16x32_bf16 v[64:67], a[212:215], a[60:63], v[64:67]\n"
      "global_load_lds_dwordx4 v219, s[14:15] offset:3072\n"
      "v_mfma_f32_16x16x32_bf16 v[64:67], a[208:211], a[56:59], v[64:67]\n"
      "s_add_u32 s3, s55, s20\n"
      "ds_read_b64_tr_b16 a[186:187], v237 offset:33280\n"
      "v_mfma_f32_16x16x32_bf16 v[68:71], a[208:211], a[64:67], v[68:71]\n"
      "s_add_u32 s14, s4, s3\n"
      "s_addc_u32 s15, s5, 0\n"
      "v_mfma_f32_16x16x32_bf16 v[68:71], a[212:215], a[68:71], v[68:71]\n"
      "s_add_u32 m0, s29, 114688\n"
      "ds_read_b64_tr_b16 a[188:189], v237 offset:36864\n"
      "v_mfma_f32_16x16x32_bf16 v[72:75], a[216:219], a[48:51], v[72:75]\n"
      "ds_read_b64_tr_b16 a[190:191], v237 offset:37376\n"
      "ds_read_b64_tr_b16 a[192:193], v238 offset:32768\n"
      "v_mfma_f32_16x16x32_bf16 v[72:75], a[220:223], a[52:55], v[72:75]\n"
      "ds_read_b64_tr_b16 a[194:195], v238 offset:33280\n"
      "ds_read_b64_tr_b16 a[196:197], v238 offset:36864\n"
      "v_mfma_f32_16x16x32_bf16 v[76:79], a[220:223], a[60:63], v[76:79]\n"
      "global_load_lds_dwordx4 v220, s[16:17] offset:0\n"
      "v_mfma_f32_16x16x32_bf16 v[76:79], a[216:219], a[56:59], v[76:79]\n"
      "ds_read_b64_tr_b16 a[198:199], v238 offset:37376\n"
      "ds_read_b128 a[72:75], v239 offset:36864\n"
      "v_mfma_f32_16x16x32_bf16 v[80:83], a[216:219], a[64:67], v[80:83]\n"
      "global_load_lds_dwordx4 v221, s[16:17] offset:1024\n"
      "v_mfma_f32_16x16x32_bf16 v[80:83], a[220:223], a[68:71], v[80:83]\n"
      "ds_read_b128 a[76:79], v240 offset:36864\n"
      "ds_read_b128 a[80:83], v239 offset:38912\n"
      "v_mfma_f32_16x16x32_bf16 v[84:87], a[224:227], a[48:51], v[84:87]\n"
      "global_load_lds_dwordx4 v222, s[16:17] offset:2048\n"
      "v_mfma_f32_16x16x32_bf16 v[84:87], a[228:231], a[52:55], v[84:87]\n"
      "ds_read_b128 a[84:87], v240 offset:38912\n"
      "v_mfma_f32_16x16x32_bf16 v[88:91], a[228:231], a[60:63], v[88:91]\n"
      "ds_read_b128 a[88:91], v239 offset:40960\n"
      "ds_read_b128 a[92:95], v240 offset:40960\n"
      "v_mfma_f32_16x16x32_bf16 v[88:91], a[224:227], a[56:59], v[88:91]\n"
      "s_add_u32 s16, s6, s64\n"
      "s_addc_u32 s17, s7, 0\n"
      "v_mfma_f32_16x16x32_bf16 v[92:95], a[224:227], a[64:67], v[92:95]\n"
      "s_waitcnt vmcnt(17) lgkmcnt(0)\n"
      "v_mfma_f32_16x16x32_bf16 v[92:95], a[228:231], a[68:71], v[92:95]\n"
      "s_barrier\n"
      "v_mfma_f32_16x16x32_bf16 v[96:99], a[168:171], a[48:51], v[96:99]\n"
      "s_add_u32 m0, s28, 32768\n"
      "ds_read_b64_tr_b16 a[200:201], v235 offset:49152\n"
      "v_mfma_f32_16x16x32_bf16 v[96:99], a[172:175], a[52:55], v[96:99]\n"
      "ds_read_b64_tr_b16 a[202:203], v235 offset:49664\n"
      "v_mfma_f32_16x16x32_bf16 v[100:103], a[172:175], a[60:63], v[100:103]\n"
      "ds_read_b64_tr_b16 a[204:205], v235 offset:53248\n"
      "v_mfma_f32_16x16x32_bf16 v[100:103], a[168:171], a[56:59], v[100:103]\n"
      "global_load_lds_dwordx4 v216, s[14:15] offset:0\n"
      "v_mfma_f32_16x16x32_bf16 v[104:107], a[168:171], a[64:67], v[104:107]\n"
      "ds_read_b64_tr_b16 a[206:207], v235 offset:53760\n"
      "v_mfma_f32_16x16x32_bf16 v[104:107], a[172:175], a[68:71], v[104:107]\n"
      "ds_read_b64_tr_b16 a[208:209], v236 offset:49152\n"
      "v_mfma_f32_16x16x32_bf16 v[108:111], a[176:179], a[48:51], v[108:111]\n"
      "ds_read_b64_tr_b16 a[210:211], v236 offset:49664\n"
      "v_mfma_f32_16x16x32_bf16 v[108:111], a[180:183], a[52:55], v[108:111]\n"
      "global_load_lds_dwordx4 v217, s[14:15] offset:1024\n"
      "v_mfma_f32_16x16x32_bf16 v[112:115], a[180:183], a[60:63], v[112:115]\n"
      "ds_read_b64_tr_b16 a[212:213], v236 offset:53248\n"
      "v_mfma_f32_16x16x32_bf16 v[112:115], a[176:179], a[56:59], v[112:115]\n"
      "ds_read_b64_tr_b16 a[214:215], v236 offset:53760\n"
      "ds_read_b64_tr_b16 a[216:217], v237 offset:49152\n"
      "v_mfma_f32_16x16x32_bf16 v[116:119], a[176:179], a[64:67], v[116:119]\n"
      "global_load_lds_dwordx4 v218, s[14:15] offset:2048\n"
      "v_mfma_f32_16x16x32_bf16 v[116:119], a[180:183], a[68:71], v[116:119]\n"
      "ds_read_b64_tr_b16 a[218:219], v237 offset:49664\n"
      "v_mfma_f32_16x16x32_bf16 v[120:123], a[184:187], a[48:51], v[120:123]\n"
      "ds_read_b64_tr_b16 a[220:221], v237 offset:53248\n"
      "v_mfma_f32_16x16x32_bf16 v[120:123], a[188:191], a[52:55], v[120:123]\n"
      "ds_read_b64_tr_b16 a[222:223], v237 offset:53760\n"
      "v_mfma_f32_16x16x32_bf16 v[124:127], a[188:191], a[60:63], v[124:127]\n"
      "global_load_lds_dwordx4 v219, s[14:15] offset:3072\n"
      "v_mfma_f32_16x16x32_bf16 v[124:127], a[184:187], a[56:59], v[124:127]\n"
      "ds_read_b64_tr_b16 a[224:225], v238 offset:49152\n"
      "v_mfma_f32_16x16x32_bf16 v[128:131], a[184:187], a[64:67], v[128:131]\n"
      "ds_read_b64_tr_b16 a[226:227], v238 offset:49664\n"
      "v_mfma_f32_16x16x32_bf16 v[128:131], a[188:191], a[68:71], v[128:131]\n"
      "s_add_u32 s3, s55, s21\n"
      "ds_read_b64_tr_b16 a[228:229], v238 offset:53248\n"
      "v_mfma_f32_16x16x32_bf16 v[132:135], a[192:195], a[48:51], v[132:135]\n"
      "ds_read_b64_tr_b16 a[230:231], v238 offset:53760\n"
      "v_mfma_f32_16x16x32_bf16 v[132:135], a[196:199], a[52:55], v[132:135]\n"
      "v_mfma_f32_16x16x32_bf16 v[136:139], a[196:199], a[60:63], v[136:139]\n"
      "s_add_u32 s14, s4, s3\n"
      "s_addc_u32 s15, s5, 0\n"
      "v_mfma_f32_16x16x32_bf16 v[136:139], a[192:195], a[56:59], v[136:139]\n"
      "v_mfma_f32_16x16x32_bf16 v[140:143], a[192:195], a[64:67], v[140:143]\n"
      "s_waitcnt vmcnt(11) lgkmcnt(0)\n"
      "v_mfma_f32_16x16x32_bf16 v[140:143], a[196:199], a[68:71], v[140:143]\n"
      "s_barrier\n"
      "v_mfma_f32_16x16x32_bf16 v[144:147], a[200:203], a[48:51], v[144:147]\n"
      "s_add_u32 m0, s28, 49152\n"
      "ds_read_b64_tr_b16 a[168:169], v235 offset:0\n"
      "v_mfma_f32_16x16x32_bf16 v[144:147], a[204:207], a[52:55], v[144:147]\n"
      "ds_read_b64_tr_b16 a[170:171], v235 offset:512\n"
      "ds_read_b64_tr_b16 a[172:173], v235 offset:4096\n"
      "v_mfma_f32_16x16x32_bf16 v[148:151], a[204:207], a[60:63], v[148:151]\n"
      "global_load_lds_dwordx4 v216, s[14:15] offset:0\n"
      "v_mfma_f32_16x16x32_bf16 v[148:151], a[200:203], a[56:59], v[148:151]\n"
      "ds_read_b64_tr_b16 a[174:175], v235 offset:4608\n"
      "v_mfma_f32_16x16x32_bf16 v[152:155], a[200:203], a[64:67], v[152:155]\n"
      "global_load_lds_dwordx4 v217, s[14:15] offset:1024\n"
      "v_mfma_f32_16x16x32_bf16 v[152:155], a[204:207], a[68:71], v[152:155]\n"
      "ds_read_b64_tr_b16 a[176:177], v236 offset:0\n"
      "ds_read_b64_tr_b16 a[178:179], v236 offset:512\n"
      "v_mfma_f32_16x16x32_bf16 v[156:159], a[208:211], a[48:51], v[156:159]\n"
      "global_load_lds_dwordx4 v218, s[14:15] offset:2048\n"
      "v_mfma_f32_16x16x32_bf16 v[156:159], a[212:215], a[52:55], v[156:159]\n"
      "ds_read_b64_tr_b16 a[180:181], v236 offset:4096\n"
      "v_mfma_f32_16x16x32_bf16 v[160:163], a[212:215], a[60:63], v[160:163]\n"
      "global_load_lds_dwordx4 v219, s[14:15] offset:3072\n"
      "v_mfma_f32_16x16x32_bf16 v[160:163], a[208:211], a[56:59], v[160:163]\n"
      "s_add_u32 s3, s56, s18\n"
      "ds_read_b64_tr_b16 a[182:183], v236 offset:4608\n"
      "v_mfma_f32_16x16x32_bf16 v[164:167], a[208:211], a[64:67], v[164:167]\n"
      "ds_read_b64_tr_b16 a[184:185], v237 offset:0\n"
      "ds_read_b64_tr_b16 a[186:187], v237 offset:512\n"
      "v_mfma_f32_16x16x32_bf16 v[164:167], a[212:215], a[68:71], v[164:167]\n"
      "s_add_u32 s14, s4, s3\n"
      "s_addc_u32 s15, s5, 0\n"
      "v_mfma_f32_16x16x32_bf16 v[168:171], a[216:219], a[48:51], v[168:171]\n"
      "ds_read_b64_tr_b16 a[188:189], v237 offset:4096\n"
      "ds_read_b64_tr_b16 a[190:191], v237 offset:4608\n"
      "v_mfma_f32_16x16x32_bf16 v[168:171], a[220:223], a[52:55], v[168:171]\n"
      "s_add_u32 m0, s29, 126976\n"
      "ds_read_b64_tr_b16 a[192:193], v238 offset:0\n"
      "v_mfma_f32_16x16x32_bf16 v[172:175], a[220:223], a[60:63], v[172:175]\n"
      "global_load_lds_dwordx4 v220, s[16:17] offset:0\n"
      "v_mfma_f32_16x16x32_bf16 v[172:175], a[216:219], a[56:59], v[172:175]\n"
      "ds_read_b64_tr_b16 a[194:195], v238 offset:512\n"
      "v_mfma_f32_16x16x32_bf16 v[176:179], a[216:219], a[64:67], v[176:179]\n"
      "global_load_lds_dwordx4 v221, s[16:17] offset:1024\n"
      "v_mfma_f32_16x16x32_bf16 v[176:179], a[220:223], a[68:71], v[176:179]\n"
      "ds_read_b64_tr_b16 a[196:197], v238 offset:4096\n"
      "ds_read_b64_tr_b16 a[198:199], v238 offset:4608\n"
      "v_mfma_f32_16x16x32_bf16 v[180:183], a[224:227], a[48:51], v[180:183]\n"
      "global_load_lds_dwordx4 v222, s[16:17] offset:2048\n"
      "v_mfma_f32_16x16x32_bf16 v[180:183], a[228:231], a[52:55], v[180:183]\n"
      "v_mfma_f32_16x16x32_bf16 v[184:187], a[228:231], a[60:63], v[184:187]\n"
      "s_add_u32 s16, s8, s19\n"
      "s_addc_u32 s17, s9, 0\n"
      "v_mfma_f32_16x16x32_bf16 v[184:187], a[224:227], a[56:59], v[184:187]\n"
      "v_mfma_f32_16x16x32_bf16 v[188:191], a[224:227], a[64:67], v[188:191]\n"
      "s_waitcnt vmcnt(14) lgkmcnt(0)\n"
      "v_mfma_f32_16x16x32_bf16 v[188:191], a[228:231], a[68:71], v[188:191]\n"
      "s_barrier\n"
      "v_mfma_f32_16x16x32_bf16 v[0:3], a[168:171], a[72:75], v[0:3]\n"
      "s_add_u32 m0, s28, 0\n"
      "ds_read_b64_tr_b16 a[200:201], v235 offset:16384\n"
      "v_mfma_f32_16x16x32_bf16 v[0:3], a[172:175], a[76:79], v[0:3]\n"
      "ds_read_b64_tr_b16 a[202:203], v235 offset:16896\n"
      "v_mfma_f32_16x16x32_bf16 v[4:7], a[172:175], a[84:87], v[4:7]\n"
      "ds_read_b64_tr_b16 a[204:205], v235 offset:20480\n"
      "v_mfma_f32_16x16x32_bf16 v[4:7], a[168:171], a[80:83], v[4:7]\n"
      "global_load_lds_dwordx4 v216, s[14:15] offset:0\n"
      "v_mfma_f32_16x16x32_bf16 v[8:11], a[168:171], a[88:91], v[8:11]\n"
      "ds_read_b64_tr_b16 a[206:207], v235 offset:20992\n"
      "v_mfma_f32_16x16x32_bf16 v[8:11], a[172:175], a[92:95], v[8:11]\n"
      "ds_read_b64_tr_b16 a[208:209], v236 offset:16384\n"
      "v_mfma_f32_16x16x32_bf16 v[12:15], a[176:179], a[72:75], v[12:15]\n"
      "ds_read_b64_tr_b16 a[210:211], v236 offset:16896\n"
      "v_mfma_f32_16x16x32_bf16 v[12:15], a[180:183], a[76:79], v[12:15]\n"
      "global_load_lds_dwordx4 v217, s[14:15] offset:1024\n"
      "v_mfma_f32_16x16x32_bf16 v[16:19], a[180:183], a[84:87], v[16:19]\n"
      "ds_read_b64_tr_b16 a[212:213], v236 offset:20480\n"
      "v_mfma_f32_16x16x32_bf16 v[16:19], a[176:179], a[80:83], v[16:19]\n"
      "ds_read_b64_tr_b16 a[214:215], v236 offset:20992\n"
      "ds_read_b64_tr_b16 a[216:217], v237 offset:16384\n"
      "v_mfma_f32_16x16x32_bf16 v[20:23], a[176:179], a[88:91], v[20:23]\n"
      "global_load_lds_dwordx4 v218, s[14:15] offset:2048\n"
      "v_mfma_f32_16x16x32_bf16 v[20:23], a[180:183], a[92:95], v[20:23]\n"
      "ds_read_b64_tr_b16 a[218:219], v237 offset:16896\n"
      "v_mfma_f32_16x16x32_bf16 v[24:27], a[184:187], a[72:75], v[24:27]\n"
      "ds_read_b64_tr_b16 a[220:221], v237 offset:20480\n"
      "v_mfma_f32_16x16x32_bf16 v[24:27], a[188:191], a[76:79], v[24:27]\n"
      "ds_read_b64_tr_b16 a[222:223], v237 offset:20992\n"
      "v_mfma_f32_16x16x32_bf16 v[28:31], a[188:191], a[84:87], v[28:31]\n"
      "global_load_lds_dwordx4 v219, s[14:15] offset:3072\n"
      "v_mfma_f32_16x16x32_bf16 v[28:31], a[184:187], a[80:83], v[28:31]\n"
      "ds_read_b64_tr_b16 a[224:225], v238 offset:16384\n"
      "v_mfma_f32_16x16x32_bf16 v[32:35], a[184:187], a[88:91], v[32:35]\n"
      "ds_read_b64_tr_b16 a[226:227], v238 offset:16896\n"
      "v_mfma_f32_16x16x32_bf16 v[32:35], a[188:191], a[92:95], v[32:35]\n"
      "s_add_u32 s3, s56, s19\n"
      "ds_read_b64_tr_b16 a[228:229], v238 offset:20480\n"
      "v_mfma_f32_16x16x32_bf16 v[36:39], a[192:195], a[72:75], v[36:39]\n"
      "ds_read_b64_tr_b16 a[230:231], v238 offset:20992\n"
      "v_mfma_f32_16x16x32_bf16 v[36:39], a[196:199], a[76:79], v[36:39]\n"
      "v_mfma_f32_16x16x32_bf16 v[40:43], a[196:199], a[84:87], v[40:43]\n"
      "s_add_u32 s14, s4, s3\n"
      "s_addc_u32 s15, s5, 0\n"
      "v_mfma_f32_16x16x32_bf16 v[40:43], a[192:195], a[80:83], v[40:43]\n"
      "v_mfma_f32_16x16x32_bf16 v[44:47], a[192:195], a[88:91], v[44:47]\n"
      "s_waitcnt vmcnt(11) lgkmcnt(0)\n"
      "v_mfma_f32_16x16x32_bf16 v[44:47], a[196:199], a[92:95], v[44:47]\n"
      "s_barrier\n"
      "v_mfma_f32_16x16x32_bf16 v[48:51], a[200:203], a[72:75], v[48:51]\n"
      "s_add_u32 m0, s28, 16384\n"
      "ds_read_b64_tr_b16 a[168:169], v235 offset:32768\n"
      "ds_read_b64_tr_b16 a[170:171], v235 offset:33280\n"
      "ds_read_b64_tr_b16 a[172:173], v235 offset:36864\n"
      "ds_read_b64_tr_b16 a[174:175], v235 offset:37376\n"
      "ds_read_b64_tr_b16 a[176:177], v236 offset:32768\n"
      "ds_read_b64_tr_b16 a[178:179], v236 offset:33280\n"
      "v_mfma_f32_16x16x32_bf16 v[48:51], a[204:207], a[76:79], v[48:51]\n"
      "global_load_lds_dwordx4 v216, s[14:15] offset:0\n"
      "v_mfma_f32_16x16x32_bf16 v[52:55], a[204:207], a[84:87], v[52:55]\n"
      "global_load_lds_dwordx4 v217, s[14:15] offset:1024\n"
      "v_mfma_f32_16x16x32_bf16 v[52:55], a[200:203], a[80:83], v[52:55]\n"
      "ds_read_b64_tr_b16 a[180:181], v236 offset:36864\n"
      "ds_read_b64_tr_b16 a[182:183], v236 offset:37376\n"
      "v_mfma_f32_16x16x32_bf16 v[56:59], a[200:203], a[88:91], v[56:59]\n"
      "global_load_lds_dwordx4 v218, s[14:15] offset:2048\n"
      "v_mfma_f32_16x16x32_bf16 v[56:59], a[204:207], a[92:95], v[56:59]\n"
      "global_load_lds_dwordx4 v219, s[14:15] offset:3072\n"
      "v_mfma_f32_16x16x32_bf16 v[60:63], a[208:211], a[72:75], v[60:63]\n"
      "s_add_u32 s3, s56, s20\n"
      "ds_read_b64_tr_b16 a[184:185], v237 offset:32768\n"
      "v_mfma_f32_16x16x32_bf16 v[60:63], a[212:215], a[76:79], v[60:63]\n"
      "s_add_u32 s14, s4, s3\n"
      "s_addc_u32 s15, s5, 0\n"
      "ds_read_b64_tr_b16 a[186:187], v237 offset:33280\n"
      "v_mfma_f32_16x16x32_bf16 v[64:67], a[212:215], a[84:87], v[64:67]\n"
      "ds_read_b64_tr_b16 a[188:189], v237 offset:36864\n"
      "ds_read_b64_tr_b16 a[190:191], v237 offset:37376\n"
      "v_mfma_f32_16x16x32_bf16 v[64:67], a[208:211], a[80:83], v[64:67]\n"
      "s_add_u32 m0, s30, 90112\n"
      "ds_read_b64_tr_b16 a[192:193], v238 offset:32768\n"
      "v_mfma_f32_16x16x32_bf16 v[68:71], a[208:211], a[88:91], v[68:71]\n"
      "global_load_lds_dwordx4 v223, s[16:17] offset:0 nt\n"
      "v_mfma_f32_16x16x32_bf16 v[68:71], a[212:215], a[92:95], v[68:71]\n"
      "global_load_lds_dwordx4 v224, s[16:17] offset:1024 nt\n"
      "v_mfma_f32_16x16x32_bf16 v[72:75], a[216:219], a[72:75], v[72:75]\n"
      "ds_read_b64_tr_b16 a[194:195], v238 offset:33280\n"
      "ds_read_b64_tr_b16 a[196:197], v238 offset:36864\n"
      "ds_read_b64_tr_b16 a[198:199], v238 offset:37376\n"
      "v_mfma_f32_16x16x32_bf16 v[72:75], a[220:223], a[76:79], v[72:75]\n"
      "global_load_lds_dwordx4 v225, s[16:17] offset:2048 nt\n"
      "v_mfma_f32_16x16x32_bf16 v[76:79], a[220:223], a[84:87], v[76:79]\n"
      "global_load_lds_dwordx4 v226, s[16:17] offset:3072 nt\n"
      "v_mfma_f32_16x16x32_bf16 v[76:79], a[216:219], a[80:83], v[76:79]\n"
      "s_add_u32 s16, s16, 16384\n"
      "s_addc_u32 s17, s17, 0\n"
      "v_add_u32 v239, 49152, v239\n"
      "v_add_u32 v240, 49152, v240\n"
      "ds_read_b128 a[96:99], v239 offset:0\n"
      "v_mfma_f32_16x16x32_bf16 v[80:83], a[216:219], a[88:91], v[80:83]\n"
      "s_add_u32 m0, s30, 94208\n"
      "ds_read_b128 a[100:103], v240 offset:0\n"
      "v_mfma_f32_16x16x32_bf16 v[80:83], a[220:223], a[92:95], v[80:83]\n"
      "ds_read_b128 a[104:107], v239 offset:2048\n"
      "ds_read_b128 a[108:111], v240 offset:2048\n"
      "v_mfma_f32_16x16x32_bf16 v[84:87], a[224:227], a[72:75], v[84:87]\n"
      "global_load_lds_dwordx4 v223, s[16:17] offset:0 nt\n"
      "v_mfma_f32_16x16x32_bf16 v[84:87], a[228:231], a[76:79], v[84:87]\n"
      "global_load_lds_dwordx4 v224, s[16:17] offset:1024 nt\n"
      "v_mfma_f32_16x16x32_bf16 v[88:91], a[228:231], a[84:87], v[88:91]\n"
      "ds_read_b128 a[112:115], v239 offset:4096\n"
      "ds_read_b128 a[116:119], v240 offset:4096\n"
      "v_mfma_f32_16x16x32_bf16 v[88:91], a[224:227], a[80:83], v[88:91]\n"
      "s_add_u32 s16, s6, s65\n"
      "s_addc_u32 s17, s7, 0\n"
      "v_mfma_f32_16x16x32_bf16 v[92:95], a[224:227], a[88:91], v[92:95]\n"
      "s_waitcnt vmcnt(17) lgkmcnt(0)\n"
      "v_mfma_f32_16x16x32_bf16 v[92:95], a[228:231], a[92:95], v[92:95]\n"
      "s_barrier\n"
      "v_mfma_f32_16x16x32_bf16 v[96:99], a[168:171], a[72:75], v[96:99]\n"
      "s_add_u32 m0, s28, 32768\n"
      "ds_read_b64_tr_b16 a[200:201], v235 offset:49152\n"
      "v_mfma_f32_16x16x32_bf16 v[96:99], a[172:175], a[76:79], v[96:99]\n"
      "ds_read_b64_tr_b16 a[202:203], v235 offset:49664\n"
      "v_mfma_f32_16x16x32_bf16 v[100:103], a[172:175], a[84:87], v[100:103]\n"
      "ds_read_b64_tr_b16 a[204:205], v235 offset:53248\n"
      "v_mfma_f32_16x16x32_bf16 v[100:103], a[168:171], a[80:83], v[100:103]\n"
      "global_load_lds_dwordx4 v216, s[14:15] offset:0\n"
      "v_mfma_f32_16x16x32_bf16 v[104:107], a[168:171], a[88:91], v[104:107]\n"
      "ds_read_b64_tr_b16 a[206:207], v235 offset:53760\n"
      "v_mfma_f32_16x16x32_bf16 v[104:107], a[172:175], a[92:95], v[104:107]\n"
      "ds_read_b64_tr_b16 a[208:209], v236 offset:49152\n"
      "v_mfma_f32_16x16x32_bf16 v[108:111], a[176:179], a[72:75], v[108:111]\n"
      "ds_read_b64_tr_b16 a[210:211], v236 offset:49664\n"
      "v_mfma_f32_16x16x32_bf16 v[108:111], a[180:183], a[76:79], v[108:111]\n"
      "global_load_lds_dwordx4 v217, s[14:15] offset:1024\n"
      "v_mfma_f32_16x16x32_bf16 v[112:115], a[180:183], a[84:87], v[112:115]\n"
      "ds_read_b64_tr_b16 a[212:213], v236 offset:53248\n"
      "v_mfma_f32_16x16x32_bf16 v[112:115], a[176:179], a[80:83], v[112:115]\n"
      "ds_read_b64_tr_b16 a[214:215], v236 offset:53760\n"
      "ds_read_b64_tr_b16 a[216:217], v237 offset:49152\n"
      "v_mfma_f32_16x16x32_bf16 v[116:119], a[176:179], a[88:91], v[116:119]\n"
      "global_load_lds_dwordx4 v218, s[14:15] offset:2048\n"
      "v_mfma_f32_16x16x32_bf16 v[116:119], a[180:183], a[92:95], v[116:119]\n"
      "ds_read_b64_tr_b16 a[218:219], v237 offset:49664\n"
      "v_mfma_f32_16x16x32_bf16 v[120:123], a[184:187], a[72:75], v[120:123]\n"
      "ds_read_b64_tr_b16 a[220:221], v237 offset:53248\n"
      "v_mfma_f32_16x16x32_bf16 v[120:123], a[188:191], a[76:79], v[120:123]\n"
      "ds_read_b64_tr_b16 a[222:223], v237 offset:53760\n"
      "v_mfma_f32_16x16x32_bf16 v[124:127], a[188:191], a[84:87], v[124:127]\n"
      "global_load_lds_dwordx4 v219, s[14:15] offset:3072\n"
      "v_mfma_f32_16x16x32_bf16 v[124:127], a[184:187], a[80:83], v[124:127]\n"
      "ds_read_b64_tr_b16 a[224:225], v238 offset:49152\n"
      "v_mfma_f32_16x16x32_bf16 v[128:131], a[184:187], a[88:91], v[128:131]\n"
      "ds_read_b64_tr_b16 a[226:227], v238 offset:49664\n"
      "v_mfma_f32_16x16x32_bf16 v[128:131], a[188:191], a[92:95], v[128:131]\n"
      "s_add_u32 s3, s56, s21\n"
      "ds_read_b64_tr_b16 a[228:229], v238 offset:53248\n"
      "v_mfma_f32_16x16x32_bf16 v[132:135], a[192:195], a[72:75], v[132:135]\n"
      "ds_read_b64_tr_b16 a[230:231], v238 offset:53760\n"
      "v_mfma_f32_16x16x32_bf16 v[132:135], a[196:199], a[76:79], v[132:135]\n"
      "v_mfma_f32_16x16x32_bf16 v[136:139], a[196:199], a[84:87], v[136:139]\n"
      "s_add_u32 s14, s4, s3\n"
      "s_addc_u32 s15, s5, 0\n"
      "v_mfma_f32_16x16x32_bf16 v[136:139], a[192:195], a[80:83], v[136:139]\n"
      "v_mfma_f32_16x16x32_bf16 v[140:143], a[192:195], a[88:91], v[140:143]\n"
      "s_waitcnt vmcnt(14) lgkmcnt(0)\n"
      "v_mfma_f32_16x16x32_bf16 v[140:143], a[196:199], a[92:95], v[140:143]\n"
      "s_barrier\n"
      "v_mfma_f32_16x16x32_bf16 v[144:147], a[200:203], a[72:75], v[144:147]\n"
      "s_add_u32 m0, s28, 49152\n"
      "ds_read_b64_tr_b16 a[168:169], v235 offset:0\n"
      "v_mfma_f32_16x16x32_bf16 v[144:147], a[204:207], a[76:79], v[144:147]\n"
      "ds_read_b64_tr_b16 a[170:171], v235 offset:512\n"
      "v_mfma_f32_16x16x32_bf16 v[148:151], a[204:207], a[84:87], v[148:151]\n"
      "ds_read_b64_tr_b16 a[172:173], v235 offset:4096\n"
      "ds_read_b64_tr_b16 a[174:175], v235 offset:4608\n"
      "v_mfma_f32_16x16x32_bf16 v[148:151], a[200:203], a[80:83], v[148:151]\n"
      "global_load_lds_dwordx4 v216, s[14:15] offset:0\n"
      "v_mfma_f32_16x16x32_bf16 v[152:155], a[200:203], a[88:91], v[152:155]\n"
      "ds_read_b64_tr_b16 a[176:177], v236 offset:0\n"
      "v_mfma_f32_16x16x32_bf16 v[152:155], a[204:207], a[92:95], v[152:155]\n"
      "ds_read_b64_tr_b16 a[178:179], v236 offset:512\n"
      "ds_read_b64_tr_b16 a[180:181], v236 offset:4096\n"
      "v_mfma_f32_16x16x32_bf16 v[156:159], a[208:211], a[72:75], v[156:159]\n"
      "ds_read_b64_tr_b16 a[182:183], v236 offset:4608\n"
      "v_mfma_f32_16x16x32_bf16 v[156:159], a[212:215], a[76:79], v[156:159]\n"
      "global_load_lds_dwordx4 v217, s[14:15] offset:1024\n"
      "v_mfma_f32_16x16x32_bf16 v[160:163], a[212:215], a[84:87], v[160:163]\n"
      "ds_read_b64_tr_b16 a[184:185], v237 offset:0\n"
      "ds_read_b64_tr_b16 a[186:187], v237 offset:512\n"
      "v_mfma_f32_16x16x32_bf16 v[160:163], a[208:211], a[80:83], v[160:163]\n"
      "ds_read_b64_tr_b16 a[188:189], v237 offset:4096\n"
      "v_mfma_f32_16x16x32_bf16 v[164:167], a[208:211], a[88:91], v[164:167]\n"
      "global_load_lds_dwordx4 v218, s[14:15] offset:2048\n"
      "v_mfma_f32_16x16x32_bf16 v[164:167], a[212:215], a[92:95], v[164:167]\n"
      "ds_read_b64_tr_b16 a[190:191], v237 offset:4608\n"
      "v_mfma_f32_16x16x32_bf16 v[168:171], a[216:219], a[72:75], v[168:171]\n"
      "ds_read_b64_tr_b16 a[192:193], v238 offset:0\n"
      "ds_read_b64_tr_b16 a[194:195], v238 offset:512\n"
      "v_mfma_f32_16x16x32_bf16 v[168:171], a[220:223], a[76:79], v[168:171]\n"
      "ds_read_b64_tr_b16 a[196:197], v238 offset:4096\n"
      "v_mfma_f32_16x16x32_bf16 v[172:175], a[220:223], a[84:87], v[172:175]\n"
      "global_load_lds_dwordx4 v219, s[14:15] offset:3072\n"
      "v_mfma_f32_16x16x32_bf16 v[172:175], a[216:219], a[80:83], v[172:175]\n"
      "ds_read_b64_tr_b16 a[198:199], v238 offset:4608\n"
      "ds_read_b128 a[120:123], v239 offset:12288\n"
      "v_mfma_f32_16x16x32_bf16 v[176:179], a[216:219], a[88:91], v[176:179]\n"
      "ds_read_b128 a[124:127], v240 offset:12288\n"
      "v_mfma_f32_16x16x32_bf16 v[176:179], a[220:223], a[92:95], v[176:179]\n"
      "s_add_u32 s3, s57, s18\n"
      "ds_read_b128 a[128:131], v239 offset:14336\n"
      "v_mfma_f32_16x16x32_bf16 v[180:183], a[224:227], a[72:75], v[180:183]\n"
      "ds_read_b128 a[132:135], v240 offset:14336\n"
      "ds_read_b128 a[136:139], v239 offset:16384\n"
      "v_mfma_f32_16x16x32_bf16 v[180:183], a[228:231], a[76:79], v[180:183]\n"
      "ds_read_b128 a[140:143], v240 offset:16384\n"
      "v_mfma_f32_16x16x32_bf16 v[184:187], a[228:231], a[84:87], v[184:187]\n"
      "s_add_u32 s14, s4, s3\n"
      "s_addc_u32 s15, s5, 0\n"
      "v_mfma_f32_16x16x32_bf16 v[184:187], a[224:227], a[80:83], v[184:187]\n"
      "v_mfma_f32_16x16x32_bf16 v[188:191], a[224:227], a[88:91], v[188:191]\n"
      "s_waitcnt vmcnt(14) lgkmcnt(0)\n"
      "v_mfma_f32_16x16x32_bf16 v[188:191], a[228:231], a[92:95], v[188:191]\n"
      "s_barrier\n"
      "v_mfma_f32_16x16x32_bf16 v[0:3], a[168:171], a[96:99], v[0:3]\n"
      "s_add_u32 m0, s28, 0\n"
      "ds_read_b64_tr_b16 a[200:201], v235 offset:16384\n"
      "v_mfma_f32_16x16x32_bf16 v[0:3], a[172:175], a[100:103], v[0:3]\n"
      "ds_read_b64_tr_b16 a[202:203], v235 offset:16896\n"
      "ds_read_b64_tr_b16 a[204:205], v235 offset:20480\n"
      "v_mfma_f32_16x16x32_bf16 v[4:7], a[172:175], a[108:111], v[4:7]\n"
      "global_load_lds_dwordx4 v216, s[14:15] offset:0\n"
      "v_mfma_f32_16x16x32_bf16 v[4:7], a[168:171], a[104:107], v[4:7]\n"
      "ds_read_b64_tr_b16 a[206:207], v235 offset:20992\n"
      "v_mfma_f32_16x16x32_bf16 v[8:11], a[168:171], a[112:115], v[8:11]\n"
      "global_load_lds_dwordx4 v217, s[14:15] offset:1024\n"
      "v_mfma_f32_16x16x32_bf16 v[8:11], a[172:175], a[116:119], v[8:11]\n"
      "ds_read_b64_tr_b16 a[208:209], v236 offset:16384\n"
      "ds_read_b64_tr_b16 a[210:211], v236 offset:16896\n"
      "v_mfma_f32_16x16x32_bf16 v[12:15], a[176:179], a[96:99], v[12:15]\n"
      "global_load_lds_dwordx4 v218, s[14:15] offset:2048\n"
      "v_mfma_f32_16x16x32_bf16 v[12:15], a[180:183], a[100:103], v[12:15]\n"
      "ds_read_b64_tr_b16 a[212:213], v236 offset:20480\n"
      "v_mfma_f32_16x16x32_bf16 v[16:19], a[180:183], a[108:111], v[16:19]\n"
      "global_load_lds_dwordx4 v219, s[14:15] offset:3072\n"
      "v_mfma_f32_16x16x32_bf16 v[16:19], a[176:179], a[104:107], v[16:19]\n"
      "s_add_u32 s3, s57, s19\n"
      "ds_read_b64_tr_b16 a[214:215], v236 offset:20992\n"
      "v_mfma_f32_16x16x32_bf16 v[20:23], a[176:179], a[112:115], v[20:23]\n"
      "ds_read_b64_tr_b16 a[216:217], v237 offset:16384\n"
      "ds_read_b64_tr_b16 a[218:219], v237 offset:16896\n"
      "v_mfma_f32_16x16x32_bf16 v[20:23], a[180:183], a[116:119], v[20:23]\n"
      "s_add_u32 s14, s4, s3\n"
      "s_addc_u32 s15, s5, 0\n"
      "v_mfma_f32_16x16x32_bf16 v[24:27], a[184:187], a[96:99], v[24:27]\n"
      "ds_read_b64_tr_b16 a[220:221], v237 offset:20480\n"
      "ds_read_b64_tr_b16 a[222:223], v237 offset:20992\n"
      "v_mfma_f32_16x16x32_bf16 v[24:27], a[188:191], a[100:103], v[24:27]\n"
      "s_add_u32 m0, s29, 139264\n"
      "ds_read_b64_tr_b16 a[224:225], v238 offset:16384\n"
      "v_mfma_f32_16x16x32_bf16 v[28:31], a[188:191], a[108:111], v[28:31]\n"
      "global_load_lds_dwordx4 v220, s[16:17] offset:0\n"
      "v_mfma_f32_16x16x32_bf16 v[28:31], a[184:187], a[104:107], v[28:31]\n"
      "ds_read_b64_tr_b16 a[226:227], v238 offset:16896\n"
      "v_mfma_f32_16x16x32_bf16 v[32:35], a[184:187], a[112:115], v[32:35]\n"
      "global_load_lds_dwordx4 v221, s[16:17] offset:1024\n"
      "v_mfma_f32_16x16x32_bf16 v[32:35], a[188:191], a[116:119], v[32:35]\n"
      "ds_read_b64_tr_b16 a[228:229], v238 offset:20480\n"
      "ds_read_b64_tr_b16 a[230:231], v238 offset:20992\n"
      "v_mfma_f32_16x16x32_bf16 v[36:39], a[192:195], a[96:99], v[36:39]\n"
      "global_load_lds_dwordx4 v222, s[16:17] offset:2048\n"
      "v_mfma_f32_16x16x32_bf16 v[36:39], a[196:199], a[100:103], v[36:39]\n"
      "v_mfma_f32_16x16x32_bf16 v[40:43], a[196:199], a[108:111], v[40:43]\n"
      "s_add_u32 s16, s8, s20\n"
      "s_addc_u32 s17, s9, 0\n"
      "v_mfma_f32_16x16x32_bf16 v[40:43], a[192:195], a[104:107], v[40:43]\n"
      "v_mfma_f32_16x16x32_bf16 v[44:47], a[192:195], a[112:115], v[44:47]\n"
      "s_waitcnt vmcnt(11) lgkmcnt(0)\n"
      "v_mfma_f32_16x16x32_bf16 v[44:47], a[196:199], a[116:119], v[44:47]\n"
      "s_barrier\n"
      "v_mfma_f32_16x16x32_bf16 v[48:51], a[200:203], a[96:99], v[48:51]\n"
      "s_add_u32 m0, s28, 16384\n"
      "ds_read_b64_tr_b16 a[168:169], v235 offset:32768\n"
      "v_mfma_f32_16x16x32_bf16 v[48:51], a[204:207], a[100:103], v[48:51]\n"
      "ds_read_b64_tr_b16 a[170:171], v235 offset:33280\n"
      "v_mfma_f32_16x16x32_bf16 v[52:55], a[204:207], a[108:111], v[52:55]\n"
      "ds_read_b64_tr_b16 a[172:173], v235 offset:36864\n"
      "v_mfma_f32_16x16x32_bf16 v[52:55], a[200:203], a[104:107], v[52:55]\n"
      "global_load_lds_dwordx4 v216, s[14:15] offset:0\n"
      "v_mfma_f32_16x16x32_bf16 v[56:59], a[200:203], a[112:115], v[56:59]\n"
      "ds_read_b64_tr_b16 a[174:175], v235 offset:37376\n"
      "v_mfma_f32_16x16x32_bf16 v[56:59], a[204:207], a[116:119], v[56:59]\n"
      "ds_read_b64_tr_b16 a[176:177], v236 offset:32768\n"
      "v_mfma_f32_16x16x32_bf16 v[60:63], a[208:211], a[96:99], v[60:63]\n"
      "ds_read_b64_tr_b16 a[178:179], v236 offset:33280\n"
      "v_mfma_f32_16x16x32_bf16 v[60:63], a[212:215], a[100:103], v[60:63]\n"
      "global_load_lds_dwordx4 v217, s[14:15] offset:1024\n"
      "v_mfma_f32_16x16x32_bf16 v[64:67], a[212:215], a[108:111], v[64:67]\n"
      "ds_read_b64_tr_b16 a[180:181], v236 offset:36864\n"
      "v_mfma_f32_16x16x32_bf16 v[64:67], a[208:211], a[104:107], v[64:67]\n"
      "ds_read_b64_tr_b16 a[182:183], v236 offset:37376\n"
      "ds_read_b64_tr_b16 a[184:185], v237 offset:32768\n"
      "v_mfma_f32_16x16x32_bf16 v[68:71], a[208:211], a[112:115], v[68:71]\n"
      "global_load_lds_dwordx4 v218, s[14:15] offset:2048\n"
      "v_mfma_f32_16x16x32_bf16 v[68:71], a[212:215], a[116:119], v[68:71]\n"
      "ds_read_b64_tr_b16 a[186:187], v237 offset:33280\n"
      "v_mfma_f32_16x16x32_bf16 v[72:75], a[216:219], a[96:99], v[72:75]\n"
      "ds_read_b64_tr_b16 a[188:189], v237 offset:36864\n"
      "v_mfma_f32_16x16x32_bf16 v[72:75], a[220:223], a[100:103], v[72:75]\n"
      "ds_read_b64_tr_b16 a[190:191], v237 offset:37376\n"
      "v_mfma_f32_16x16x32_bf16 v[76:79], a[220:223], a[108:111], v[76:79]\n"
      "global_load_lds_dwordx4 v219, s[14:15] offset:3072\n"
      "v_mfma_f32_16x16x32_bf16 v[76:79], a[216:219], a[104:107], v[76:79]\n"
      "ds_read_b64_tr_b16 a[192:193], v238 offset:32768\n"
      "v_mfma_f32_16x16x32_bf16 v[80:83], a[216:219], a[112:115], v[80:83]\n"
      "ds_read_b64_tr_b16 a[194:195], v238 offset:33280\n"
      "v_mfma_f32_16x16x32_bf16 v[80:83], a[220:223], a[116:119], v[80:83]\n"
      "s_add_u32 s3, s57, s20\n"
      "ds_read_b64_tr_b16 a[196:197], v238 offset:36864\n"
      "v_mfma_f32_16x16x32_bf16 v[84:87], a[224:227], a[96:99], v[84:87]\n"
      "ds_read_b64_tr_b16 a[198:199], v238 offset:37376\n"
      "v_mfma_f32_16x16x32_bf16 v[84:87], a[228:231], a[100:103], v[84:87]\n"
      "v_mfma_f32_16x16x32_bf16 v[88:91], a[228:231], a[108:111], v[88:91]\n"
      "s_add_u32 s14, s4, s3\n"
      "s_addc_u32 s15, s5, 0\n"
      "v_mfma_f32_16x16x32_bf16 v[88:91], a[224:227], a[104:107], v[88:91]\n"
      "v_mfma_f32_16x16x32_bf16 v[92:95], a[224:227], a[112:115], v[92:95]\n"
      "s_waitcnt vmcnt(11) lgkmcnt(0)\n"
      "v_mfma_f32_16x16x32_bf16 v[92:95], a[228:231], a[116:119], v[92:95]\n"
      "s_barrier\n"
      "v_mfma_f32_16x16x32_bf16 v[96:99], a[168:171], a[96:99], v[96:99]\n"
      "s_add_u32 m0, s28, 32768\n"
      "ds_read_b64_tr_b16 a[200:201], v235 offset:49152\n"
      "ds_read_b64_tr_b16 a[202:203], v235 offset:49664\n"
      "v_mfma_f32_16x16x32_bf16 v[96:99], a[172:175], a[100:103], v[96:99]\n"
      "global_load_lds_dwordx4 v216, s[14:15] offset:0\n"
      "v_mfma_f32_16x16x32_bf16 v[100:103], a[172:175], a[108:111], v[100:103]\n"
      "global_load_lds_dwordx4 v217, s[14:15] offset:1024\n"
      "v_mfma_f32_16x16x32_bf16 v[100:103], a[168:171], a[104:107], v[100:103]\n"
      "ds_read_b64_tr_b16 a[204:205], v235 offset:53248\n"
      "ds_read_b64_tr_b16 a[206:207], v235 offset:53760\n"
      "v_mfma_f32_16x16x32_bf16 v[104:107], a[168:171], a[112:115], v[104:107]\n"
      "global_load_lds_dwordx4 v218, s[14:15] offset:2048\n"
      "v_mfma_f32_16x16x32_bf16 v[104:107], a[172:175], a[116:119], v[104:107]\n"
      "global_load_lds_dwordx4 v219, s[14:15] offset:3072\n"
      "v_mfma_f32_16x16x32_bf16 v[108:111], a[176:179], a[96:99], v[108:111]\n"
      "ds_read_b64_tr_b16 a[208:209], v236 offset:49152\n"
      "ds_read_b64_tr_b16 a[210:211], v236 offset:49664\n"
      "v_mfma_f32_16x16x32_bf16 v[108:111], a[180:183], a[100:103], v[108:111]\n"
      "s_add_u32 s3, s57, s21\n"
      "s_add_u32 s14, s4, s3\n"
      "s_addc_u32 s15, s5, 0\n"
      "v_mfma_f32_16x16x32_bf16 v[112:115], a[180:183], a[108:111], v[112:115]\n"
      "ds_read_b64_tr_b16 a[212:213], v236 offset:53248\n"
      "ds_read_b64_tr_b16 a[214:215], v236 offset:53760\n"
      "v_mfma_f32_16x16x32_bf16 v[112:115], a[176:179], a[104:107], v[112:115]\n"
      "s_add_u32 m0, s30, 114688\n"
      "ds_read_b64_tr_b16 a[216:217], v237 offset:49152\n"
      "v_mfma_f32_16x16x32_bf16 v[116:119], a[176:179], a[112:115], v[116:119]\n"
      "global_load_lds_dwordx4 v223, s[16:17] offset:0 nt\n"
      "v_mfma_f32_16x16x32_bf16 v[116:119], a[180:183], a[116:119], v[116:119]\n"
      "global_load_lds_dwordx4 v224, s[16:17] offset:1024 nt\n"
      "v_mfma_f32_16x16x32_bf16 v[120:123], a[184:187], a[96:99], v[120:123]\n"
      "ds_read_b64_tr_b16 a[218:219], v237 offset:49664\n"
      "ds_read_b64_tr_b16 a[220:221], v237 offset:53248\n"
      "v_mfma_f32_16x16x32_bf16 v[120:123], a[188:191], a[100:103], v[120:123]\n"
      "global_load_lds_dwordx4 v225, s[16:17] offset:2048 nt\n"
      "v_mfma_f32_16x16x32_bf16 v[124:127], a[188:191], a[108:111], v[124:127]\n"
      "global_load_lds_dwordx4 v226, s[16:17] offset:3072 nt\n"
      "v_mfma_f32_16x16x32_bf16 v[124:127], a[184:187], a[104:107], v[124:127]\n"
      "s_add_u32 s16, s16, 16384\n"
      "s_addc_u32 s17, s17, 0\n"
      "ds_read_b64_tr_b16 a[222:223], v237 offset:53760\n"
      "v_mfma_f32_16x16x32_bf16 v[128:131], a[184:187], a[112:115], v[128:131]\n"
      "ds_read_b64_tr_b16 a[224:225], v238 offset:49152\n"
      "ds_read_b64_tr_b16 a[226:227], v238 offset:49664\n"
      "ds_read_b64_tr_b16 a[228:229], v238 offset:53248\n"
      "v_mfma_f32_16x16x32_bf16 v[128:131], a[188:191], a[116:119], v[128:131]\n"
      "s_add_u32 m0, s30, 118784\n"
      "ds_read_b64_tr_b16 a[230:231], v238 offset:53760\n"
      "v_mfma_f32_16x16x32_bf16 v[132:135], a[192:195], a[96:99], v[132:135]\n"
      "global_load_lds_dwordx4 v223, s[16:17] offset:0 nt\n"
      "v_mfma_f32_16x16x32_bf16 v[132:135], a[196:199], a[100:103], v[132:135]\n"
      "global_load_lds_dwordx4 v224, s[16:17] offset:1024 nt\n"
      "v_mfma_f32_16x16x32_bf16 v[136:139], a[196:199], a[108:111], v[136:139]\n"
      "v_mfma_f32_16x16x32_bf16 v[136:139], a[192:195], a[104:107], v[136:139]\n"
      "s_add_u32 s16, s8, s21\n"
      "s_addc_u32 s17, s9, 0\n"
      "v_mfma_f32_16x16x32_bf16 v[140:143], a[192:195], a[112:115], v[140:143]\n"
      "s_waitcnt vmcnt(17) lgkmcnt(0)\n"
      "v_mfma_f32_16x16x32_bf16 v[140:143], a[196:199], a[116:119], v[140:143]\n"
      "s_barrier\n"
      "v_mfma_f32_16x16x32_bf16 v[144:147], a[200:203], a[96:99], v[144:147]\n"
      "s_add_u32 m0, s28, 49152\n"
      "ds_read_b64_tr_b16 a[168:169], v235 offset:0\n"
      "v_mfma_f32_16x16x32_bf16 v[144:147], a[204:207], a[100:103], v[144:147]\n"
      "ds_read_b64_tr_b16 a[170:171], v235 offset:512\n"
      "v_mfma_f32_16x16x32_bf16 v[148:151], a[204:207], a[108:111], v[148:151]\n"
      "ds_read_b64_tr_b16 a[172:173], v235 offset:4096\n"
      "v_mfma_f32_16x16x32_bf16 v[148:151], a[200:203], a[104:107], v[148:151]\n"
      "global_load_lds_dwordx4 v216, s[14:15] offset:0\n"
      "v_mfma_f32_16x16x32_bf16 v[152:155], a[200:203], a[112:115], v[152:155]\n"
      "ds_read_b64_tr_b16 a[174:175], v235 offset:4608\n"
      "v_mfma_f32_16x16x32_bf16 v[152:155], a[204:207], a[116:119], v[152:155]\n"
      "ds_read_b64_tr_b16 a[176:177], v236 offset:0\n"
      "v_mfma_f32_16x16x32_bf16 v[156:159], a[208:211], a[96:99], v[156:159]\n"
      "ds_read_b64_tr_b16 a[178:179], v236 offset:512\n"
      "v_mfma_f32_16x16x32_bf16 v[156:159], a[212:215], a[100:103], v[156:159]\n"
      "global_load_lds_dwordx4 v217, s[14:15] offset:1024\n"
      "v_mfma_f32_16x16x32_bf16 v[160:163], a[212:215], a[108:111], v[160:163]\n"
      "ds_read_b64_tr_b16 a[180:181], v236 offset:4096\n"
      "v_mfma_f32_16x16x32_bf16 v[160:163], a[208:211], a[104:107], v[160:163]\n"
      "ds_read_b64_tr_b16 a[182:183], v236 offset:4608\n"
      "ds_read_b64_tr_b16 a[184:185], v237 offset:0\n"
      "v_mfma_f32_16x16x32_bf16 v[164:167], a[208:211], a[112:115], v[164:167]\n"
      "global_load_lds_dwordx4 v218, s[14:15] offset:2048\n"
      "v_mfma_f32_16x16x32_bf16 v[164:167], a[212:215], a[116:119], v[164:167]\n"
      "ds_read_b64_tr_b16 a[186:187], v237 offset:512\n"
      "v_mfma_f32_16x16x32_bf16 v[168:171], a[216:219], a[96:99], v[168:171]\n"
      "ds_read_b64_tr_b16 a[188:189], v237 offset:4096\n"
      "v_mfma_f32_16x16x32_bf16 v[168:171], a[220:223], a[100:103], v[168:171]\n"
      "ds_read_b64_tr_b16 a[190:191], v237 offset:4608\n"
      "v_mfma_f32_16x16x32_bf16 v[172:175], a[220:223], a[108:111], v[172:175]\n"
      "global_load_lds_dwordx4 v219, s[14:15] offset:3072\n"
      "v_mfma_f32_16x16x32_bf16 v[172:175], a[216:219], a[104:107], v[172:175]\n"
      "ds_read_b64_tr_b16 a[192:193], v238 offset:0\n"
      "v_mfma_f32_16x16x32_bf16 v[176:179], a[216:219], a[112:115], v[176:179]\n"
      "ds_read_b64_tr_b16 a[194:195], v238 offset:512\n"
      "v_mfma_f32_16x16x32_bf16 v[176:179], a[220:223], a[116:119], v[176:179]\n"
      "s_add_u32 s3, s58, s18\n"
      "ds_read_b64_tr_b16 a[196:197], v238 offset:4096\n"
      "v_mfma_f32_16x16x32_bf16 v[180:183], a[224:227], a[96:99], v[180:183]\n"
      "ds_read_b64_tr_b16 a[198:199], v238 offset:4608\n"
      "v_mfma_f32_16x16x32_bf16 v[180:183], a[228:231], a[100:103], v[180:183]\n"
      "v_mfma_f32_16x16x32_bf16 v[184:187], a[228:231], a[108:111], v[184:187]\n"
      "s_add_u32 s14, s4, s3\n"
      "s_addc_u32 s15, s5, 0\n"
      "v_mfma_f32_16x16x32_bf16 v[184:187], a[224:227], a[104:107], v[184:187]\n"
      "v_mfma_f32_16x16x32_bf16 v[188:191], a[224:227], a[112:115], v[188:191]\n"
      "s_waitcnt vmcnt(14) lgkmcnt(0)\n"
      "v_mfma_f32_16x16x32_bf16 v[188:191], a[228:231], a[116:119], v[188:191]\n"
      "s_barrier\n"
      "v_mfma_f32_16x16x32_bf16 v[0:3], a[168:171], a[120:123], v[0:3]\n"
      "s_add_u32 m0, s28, 0\n"
      "ds_read_b64_tr_b16 a[200:201], v235 offset:16384\n"
      "v_mfma_f32_16x16x32_bf16 v[0:3], a[172:175], a[124:127], v[0:3]\n"
      "ds_read_b64_tr_b16 a[202:203], v235 offset:16896\n"
      "v_mfma_f32_16x16x32_bf16 v[4:7], a[172:175], a[132:135], v[4:7]\n"
      "ds_read_b64_tr_b16 a[204:205], v235 offset:20480\n"
      "ds_read_b64_tr_b16 a[206:207], v235 offset:20992\n"
      "v_mfma_f32_16x16x32_bf16 v[4:7], a[168:171], a[128:131], v[4:7]\n"
      "global_load_lds_dwordx4 v216, s[14:15] offset:0\n"
      "v_mfma_f32_16x16x32_bf16 v[8:11], a[168:171], a[136:139], v[8:11]\n"
      "ds_read_b64_tr_b16 a[208:209], v236 offset:16384\n"
      "v_mfma_f32_16x16x32_bf16 v[8:11], a[172:175], a[140:143], v[8:11]\n"
      "ds_read_b64_tr_b16 a[210:211], v236 offset:16896\n"
      "ds_read_b64_tr_b16 a[212:213], v236 offset:20480\n"
      "v_mfma_f32_16x16x32_bf16 v[12:15], a[176:179], a[120:123], v[12:15]\n"
      "ds_read_b64_tr_b16 a[214:215], v236 offset:20992\n"
      "v_mfma_f32_16x16x32_bf16 v[12:15], a[180:183], a[124:127], v[12:15]\n"
      "global_load_lds_dwordx4 v217, s[14:15] offset:1024\n"
      "v_mfma_f32_16x16x32_bf16 v[16:19], a[180:183], a[132:135], v[16:19]\n"
      "ds_read_b64_tr_b16 a[216:217], v237 offset:16384\n"
      "ds_read_b64_tr_b16 a[218:219], v237 offset:16896\n"
      "v_mfma_f32_16x16x32_bf16 v[16:19], a[176:179], a[128:131], v[16:19]\n"
      "ds_read_b64_tr_b16 a[220:221], v237 offset:20480\n"
      "v_mfma_f32_16x16x32_bf16 v[20:23], a[176:179], a[136:139], v[20:23]\n"
      "global_load_lds_dwordx4 v218, s[14:15] offset:2048\n"
      "v_mfma_f32_16x16x32_bf16 v[20:23], a[180:183], a[140:143], v[20:23]\n"
      "ds_read_b64_tr_b16 a[222:223], v237 offset:20992\n"
      "v_mfma_f32_16x16x32_bf16 v[24:27], a[184:187], a[120:123], v[24:27]\n"
      "ds_read_b64_tr_b16 a[224:225], v238 offset:16384\n"
      "ds_read_b64_tr_b16 a[226:227], v238 offset:16896\n"
      "v_mfma_f32_16x16x32_bf16 v[24:27], a[188:191], a[124:127], v[24:27]\n"
      "ds_read_b64_tr_b16 a[228:229], v238 offset:20480\n"
      "v_mfma_f32_16x16x32_bf16 v[28:31], a[188:191], a[132:135], v[28:31]\n"
      "global_load_lds_dwordx4 v219, s[14:15] offset:3072\n"
      "v_mfma_f32_16x16x32_bf16 v[28:31], a[184:187], a[128:131], v[28:31]\n"
      "ds_read_b64_tr_b16 a[230:231], v238 offset:20992\n"
      "ds_read_b128 a[144:147], v239 offset:24576\n"
      "v_mfma_f32_16x16x32_bf16 v[32:35], a[184:187], a[136:139], v[32:35]\n"
      "ds_read_b128 a[148:151], v240 offset:24576\n"
      "v_mfma_f32_16x16x32_bf16 v[32:35], a[188:191], a[140:143], v[32:35]\n"
      "s_add_u32 s3, s58, s19\n"
      "ds_read_b128 a[152:155], v239 offset:26624\n"
      "v_mfma_f32_16x16x32_bf16 v[36:39], a[192:195], a[120:123], v[36:39]\n"
      "ds_read_b128 a[156:159], v240 offset:26624\n"
      "ds_read_b128 a[160:163], v239 offset:28672\n"
      "v_mfma_f32_16x16x32_bf16 v[36:39], a[196:199], a[124:127], v[36:39]\n"
      "ds_read_b128 a[164:167], v240 offset:28672\n"
      "v_mfma_f32_16x16x32_bf16 v[40:43], a[196:199], a[132:135], v[40:43]\n"
      "s_add_u32 s14, s4, s3\n"
      "s_addc_u32 s15, s5, 0\n"
      "v_mfma_f32_16x16x32_bf16 v[40:43], a[192:195], a[128:131], v[40:43]\n"
      "v_mfma_f32_16x16x32_bf16 v[44:47], a[192:195], a[136:139], v[44:47]\n"
      "s_waitcnt vmcnt(14) lgkmcnt(0)\n"
      "v_mfma_f32_16x16x32_bf16 v[44:47], a[196:199], a[140:143], v[44:47]\n"
      "s_barrier\n"
      "v_mfma_f32_16x16x32_bf16 v[48:51], a[200:203], a[120:123], v[48:51]\n"
      "s_add_u32 m0, s28, 16384\n"
      "ds_read_b64_tr_b16 a[168:169], v235 offset:32768\n"
      "v_mfma_f32_16x16x32_bf16 v[48:51], a[204:207], a[124:127], v[48:51]\n"
      "ds_read_b64_tr_b16 a[170:171], v235 offset:33280\n"
      "v_mfma_f32_16x16x32_bf16 v[52:55], a[204:207], a[132:135], v[52:55]\n"
      "ds_read_b64_tr_b16 a[172:173], v235 offset:36864\n"
      "v_mfma_f32_16x16x32_bf16 v[52:55], a[200:203], a[128:131], v[52:55]\n"
      "global_load_lds_dwordx4 v216, s[14:15] offset:0\n"
      "v_mfma_f32_16x16x32_bf16 v[56:59], a[200:203], a[136:139], v[56:59]\n"
      "ds_read_b64_tr_b16 a[174:175], v235 offset:37376\n"
      "v_mfma_f32_16x16x32_bf16 v[56:59], a[204:207], a[140:143], v[56:59]\n"
      "ds_read_b64_tr_b16 a[176:177], v236 offset:32768\n"
      "v_mfma_f32_16x16x32_bf16 v[60:63], a[208:211], a[120:123], v[60:63]\n"
      "ds_read_b64_tr_b16 a[178:179], v236 offset:33280\n"
      "v_mfma_f32_16x16x32_bf16 v[60:63], a[212:215], a[124:127], v[60:63]\n"
      "global_load_lds_dwordx4 v217, s[14:15] offset:1024\n"
      "v_mfma_f32_16x16x32_bf16 v[64:67], a[212:215], a[132:135], v[64:67]\n"
      "ds_read_b64_tr_b16 a[180:181], v236 offset:36864\n"
      "v_mfma_f32_16x16x32_bf16 v[64:67], a[208:211], a[128:131], v[64:67]\n"
      "ds_read_b64_tr_b16 a[182:183], v236 offset:37376\n"
      "ds_read_b64_tr_b16 a[184:185], v237 offset:32768\n"
      "v_mfma_f32_16x16x32_bf16 v[68:71], a[208:211], a[136:139], v[68:71]\n"
      "global_load_lds_dwordx4 v218, s[14:15] offset:2048\n"
      "v_mfma_f32_16x16x32_bf16 v[68:71], a[212:215], a[140:143], v[68:71]\n"
      "ds_read_b64_tr_b16 a[186:187], v237 offset:33280\n"
      "v_mfma_f32_16x16x32_bf16 v[72:75], a[216:219], a[120:123], v[72:75]\n"
      "ds_read_b64_tr_b16 a[188:189], v237 offset:36864\n"
      "v_mfma_f32_16x16x32_bf16 v[72:75], a[220:223], a[124:127], v[72:75]\n"
      "ds_read_b64_tr_b16 a[190:191], v237 offset:37376\n"
      "v_mfma_f32_16x16x32_bf16 v[76:79], a[220:223], a[132:135], v[76:79]\n"
      "global_load_lds_dwordx4 v219, s[14:15] offset:3072\n"
      "v_mfma_f32_16x16x32_bf16 v[76:79], a[216:219], a[128:131], v[76:79]\n"
      "ds_read_b64_tr_b16 a[192:193], v238 offset:32768\n"
      "v_mfma_f32_16x16x32_bf16 v[80:83], a[216:219], a[136:139], v[80:83]\n"
      "ds_read_b64_tr_b16 a[194:195], v238 offset:33280\n"
      "v_mfma_f32_16x16x32_bf16 v[80:83], a[220:223], a[140:143], v[80:83]\n"
      "s_add_u32 s3, s58, s20\n"
      "ds_read_b64_tr_b16 a[196:197], v238 offset:36864\n"
      "v_mfma_f32_16x16x32_bf16 v[84:87], a[224:227], a[120:123], v[84:87]\n"
      "ds_read_b64_tr_b16 a[198:199], v238 offset:37376\n"
      "v_mfma_f32_16x16x32_bf16 v[84:87], a[228:231], a[124:127], v[84:87]\n"
      "v_mfma_f32_16x16x32_bf16 v[88:91], a[228:231], a[132:135], v[88:91]\n"
      "s_add_u32 s14, s4, s3\n"
      "s_addc_u32 s15, s5, 0\n"
      "v_mfma_f32_16x16x32_bf16 v[88:91], a[224:227], a[128:131], v[88:91]\n"
      "v_mfma_f32_16x16x32_bf16 v[92:95], a[224:227], a[136:139], v[92:95]\n"
      "s_waitcnt vmcnt(8) lgkmcnt(0)\n"
      "v_mfma_f32_16x16x32_bf16 v[92:95], a[228:231], a[140:143], v[92:95]\n"
      "s_barrier\n"
      "v_mfma_f32_16x16x32_bf16 v[96:99], a[168:171], a[120:123], v[96:99]\n"
      "s_add_u32 m0, s28, 32768\n"
      "ds_read_b64_tr_b16 a[200:201], v235 offset:49152\n"
      "ds_read_b64_tr_b16 a[202:203], v235 offset:49664\n"
      "v_mfma_f32_16x16x32_bf16 v[96:99], a[172:175], a[124:127], v[96:99]\n"
      "global_load_lds_dwordx4 v216, s[14:15] offset:0\n"
      "v_mfma_f32_16x16x32_bf16 v[100:103], a[172:175], a[132:135], v[100:103]\n"
      "global_load_lds_dwordx4 v217, s[14:15] offset:1024\n"
      "v_mfma_f32_16x16x32_bf16 v[100:103], a[168:171], a[128:131], v[100:103]\n"
      "ds_read_b64_tr_b16 a[204:205], v235 offset:53248\n"
      "ds_read_b64_tr_b16 a[206:207], v235 offset:53760\n"
      "v_mfma_f32_16x16x32_bf16 v[104:107], a[168:171], a[136:139], v[104:107]\n"
      "global_load_lds_dwordx4 v218, s[14:15] offset:2048\n"
      "v_mfma_f32_16x16x32_bf16 v[104:107], a[172:175], a[140:143], v[104:107]\n"
      "global_load_lds_dwordx4 v219, s[14:15] offset:3072\n"
      "v_mfma_f32_16x16x32_bf16 v[108:111], a[176:179], a[120:123], v[108:111]\n"
      "ds_read_b64_tr_b16 a[208:209], v236 offset:49152\n"
      "ds_read_b64_tr_b16 a[210:211], v236 offset:49664\n"
      "v_mfma_f32_16x16x32_bf16 v[108:111], a[180:183], a[124:127], v[108:111]\n"
      "s_add_u32 s3, s58, s21\n"
      "s_add_u32 s14, s4, s3\n"
      "s_addc_u32 s15, s5, 0\n"
      "v_mfma_f32_16x16x32_bf16 v[112:115], a[180:183], a[132:135], v[112:115]\n"
      "ds_read_b64_tr_b16 a[212:213], v236 offset:53248\n"
      "ds_read_b64_tr_b16 a[214:215], v236 offset:53760\n"
      "v_mfma_f32_16x16x32_bf16 v[112:115], a[176:179], a[128:131], v[112:115]\n"
      "s_add_u32 m0, s30, 139264\n"
      "ds_read_b64_tr_b16 a[216:217], v237 offset:49152\n"
      "v_mfma_f32_16x16x32_bf16 v[116:119], a[176:179], a[136:139], v[116:119]\n"
      "global_load_lds_dwordx4 v223, s[16:17] offset:0 nt\n"
      "v_mfma_f32_16x16x32_bf16 v[116:119], a[180:183], a[140:143], v[116:119]\n"
      "global_load_lds_dwordx4 v224, s[16:17] offset:1024 nt\n"
      "v_mfma_f32_16x16x32_bf16 v[120:123], a[184:187], a[120:123], v[120:123]\n"
      "ds_read_b64_tr_b16 a[218:219], v237 offset:49664\n"
      "ds_read_b64_tr_b16 a[220:221], v237 offset:53248\n"
      "v_mfma_f32_16x16x32_bf16 v[120:123], a[188:191], a[124:127], v[120:123]\n"
      "global_load_lds_dwordx4 v225, s[16:17] offset:2048 nt\n"
      "v_mfma_f32_16x16x32_bf16 v[124:127], a[188:191], a[132:135], v[124:127]\n"
      "global_load_lds_dwordx4 v226, s[16:17] offset:3072 nt\n"
      "v_mfma_f32_16x16x32_bf16 v[124:127], a[184:187], a[128:131], v[124:127]\n"
      "s_add_u32 s16, s16, 16384\n"
      "s_addc_u32 s17, s17, 0\n"
      "ds_read_b64_tr_b16 a[222:223], v237 offset:53760\n"
      "v_mfma_f32_16x16x32_bf16 v[128:131], a[184:187], a[136:139], v[128:131]\n"
      "ds_read_b64_tr_b16 a[224:225], v238 offset:49152\n"
      "ds_read_b64_tr_b16 a[226:227], v238 offset:49664\n"
      "ds_read_b64_tr_b16 a[228:229], v238 offset:53248\n"
      "v_mfma_f32_16x16x32_bf16 v[128:131], a[188:191], a[140:143], v[128:131]\n"
      "s_add_u32 m0, s30, 143360\n"
      "ds_read_b64_tr_b16 a[230:231], v238 offset:53760\n"
      "v_mfma_f32_16x16x32_bf16 v[132:135], a[192:195], a[120:123], v[132:135]\n"
      "global_load_lds_dwordx4 v223, s[16:17] offset:0 nt\n"
      "v_mfma_f32_16x16x32_bf16 v[132:135], a[196:199], a[124:127], v[132:135]\n"
      "global_load_lds_dwordx4 v224, s[16:17] offset:1024 nt\n"
      "v_mfma_f32_16x16x32_bf16 v[136:139], a[196:199], a[132:135], v[136:139]\n"
      "v_mfma_f32_16x16x32_bf16 v[136:139], a[192:195], a[128:131], v[136:139]\n"
      "s_add_u32 s16, s10, s18\n"
      "s_addc_u32 s17, s11, 0\n"
      "v_mfma_f32_16x16x32_bf16 v[140:143], a[192:195], a[136:139], v[140:143]\n"
      "s_waitcnt vmcnt(14) lgkmcnt(0)\n"
      "v_mfma_f32_16x16x32_bf16 v[140:143], a[196:199], a[140:143], v[140:143]\n"
      "s_barrier\n"
      "v_mfma_f32_16x16x32_bf16 v[144:147], a[200:203], a[120:123], v[144:147]\n"
      "ds_read_b64_tr_b16 a[168:169], v235 offset:0\n"
      "v_mfma_f32_16x16x32_bf16 v[144:147], a[204:207], a[124:127], v[144:147]\n"
      "s_add_u32 m0, s28, 49152\n"
      "ds_read_b64_tr_b16 a[170:171], v235 offset:512\n"
      "v_mfma_f32_16x16x32_bf16 v[148:151], a[204:207], a[132:135], v[148:151]\n"
      "ds_read_b64_tr_b16 a[172:173], v235 offset:4096\n"
      "v_mfma_f32_16x16x32_bf16 v[148:151], a[200:203], a[128:131], v[148:151]\n"
      "ds_read_b64_tr_b16 a[174:175], v235 offset:4608\n"
      "v_mfma_f32_16x16x32_bf16 v[152:155], a[200:203], a[136:139], v[152:155]\n"
      "global_load_lds_dwordx4 v216, s[14:15] offset:0\n"
      "v_mfma_f32_16x16x32_bf16 v[152:155], a[204:207], a[140:143], v[152:155]\n"
      "ds_read_b64_tr_b16 a[176:177], v236 offset:0\n"
      "v_mfma_f32_16x16x32_bf16 v[156:159], a[208:211], a[120:123], v[156:159]\n"
      "ds_read_b64_tr_b16 a[178:179], v236 offset:512\n"
      "v_mfma_f32_16x16x32_bf16 v[156:159], a[212:215], a[124:127], v[156:159]\n"
      "ds_read_b64_tr_b16 a[180:181], v236 offset:4096\n"
      "v_mfma_f32_16x16x32_bf16 v[160:163], a[212:215], a[132:135], v[160:163]\n"
      "ds_read_b64_tr_b16 a[182:183], v236 offset:4608\n"
      "v_mfma_f32_16x16x32_bf16 v[160:163], a[208:211], a[128:131], v[160:163]\n"
      "global_load_lds_dwordx4 v217, s[14:15] offset:1024\n"
      "v_mfma_f32_16x16x32_bf16 v[164:167], a[208:211], a[136:139], v[164:167]\n"
      "ds_read_b64_tr_b16 a[184:185], v237 offset:0\n"
      "v_mfma_f32_16x16x32_bf16 v[164:167], a[212:215], a[140:143], v[164:167]\n"
      "ds_read_b64_tr_b16 a[186:187], v237 offset:512\n"
      "v_mfma_f32_16x16x32_bf16 v[168:171], a[216:219], a[120:123], v[168:171]\n"
      "ds_read_b64_tr_b16 a[188:189], v237 offset:4096\n"
      "v_mfma_f32_16x16x32_bf16 v[168:171], a[220:223], a[124:127], v[168:171]\n"
      "ds_read_b64_tr_b16 a[190:191], v237 offset:4608\n"
      "v_mfma_f32_16x16x32_bf16 v[172:175], a[220:223], a[132:135], v[172:175]\n"
      "global_load_lds_dwordx4 v218, s[14:15] offset:2048\n"
      "v_mfma_f32_16x16x32_bf16 v[172:175], a[216:219], a[128:131], v[172:175]\n"
      "ds_read_b64_tr_b16 a[192:193], v238 offset:0\n"
      "v_mfma_f32_16x16x32_bf16 v[176:179], a[216:219], a[136:139], v[176:179]\n"
      "ds_read_b64_tr_b16 a[194:195], v238 offset:512\n"
      "v_mfma_f32_16x16x32_bf16 v[176:179], a[220:223], a[140:143], v[176:179]\n"
      "ds_read_b64_tr_b16 a[196:197], v238 offset:4096\n"
      "v_mfma_f32_16x16x32_bf16 v[180:183], a[224:227], a[120:123], v[180:183]\n"
      "ds_read_b64_tr_b16 a[198:199], v238 offset:4608\n"
      "v_mfma_f32_16x16x32_bf16 v[180:183], a[228:231], a[124:127], v[180:183]\n"
      "global_load_lds_dwordx4 v219, s[14:15] offset:3072\n"
      "v_mfma_f32_16x16x32_bf16 v[184:187], a[228:231], a[132:135], v[184:187]\n"
      "v_mfma_f32_16x16x32_bf16 v[184:187], a[224:227], a[128:131], v[184:187]\n"
      "v_mfma_f32_16x16x32_bf16 v[188:191], a[224:227], a[136:139], v[188:191]\n"
      "s_waitcnt vmcnt(14) lgkmcnt(0)\n"
      "v_mfma_f32_16x16x32_bf16 v[188:191], a[228:231], a[140:143], v[188:191]\n"
      "s_barrier\n"
      "v_mfma_f32_16x16x32_bf16 v[0:3], a[168:171], a[144:147], v[0:3]\n"
      "ds_read_b64_tr_b16 a[200:201], v235 offset:16384\n"
      "v_mfma_f32_16x16x32_bf16 v[0:3], a[172:175], a[148:151], v[0:3]\n"
      "ds_read_b64_tr_b16 a[202:203], v235 offset:16896\n"
      "v_mfma_f32_16x16x32_bf16 v[4:7], a[172:175], a[156:159], v[4:7]\n"
      "ds_read_b64_tr_b16 a[204:205], v235 offset:20480\n"
      "v_mfma_f32_16x16x32_bf16 v[4:7], a[168:171], a[152:155], v[4:7]\n"
      "v_mfma_f32_16x16x32_bf16 v[8:11], a[168:171], a[160:163], v[8:11]\n"
      "ds_read_b64_tr_b16 a[206:207], v235 offset:20992\n"
      "v_accvgpr_read_b32 v220, a232\n"
      "v_mfma_f32_16x16x32_bf16 v[8:11], a[172:175], a[164:167], v[8:11]\n"
      "ds_read_b64_tr_b16 a[208:209], v236 offset:16384\n"
      "v_accvgpr_read_b32 v221, a233\n"
      "v_mfma_f32_16x16x32_bf16 v[12:15], a[176:179], a[144:147], v[12:15]\n"
      "ds_read_b64_tr_b16 a[210:211], v236 offset:16896\n"
      "v_accvgpr_read_b32 v222, a234\n"
      "v_mfma_f32_16x16x32_bf16 v[12:15], a[180:183], a[148:151], v[12:15]\n"
      "ds_read_b64_tr_b16 a[212:213], v236 offset:20480\n"
      "v_accvgpr_read_b32 v239, a235\n"
      "v_mfma_f32_16x16x32_bf16 v[16:19], a[180:183], a[156:159], v[16:19]\n"
      "ds_read_b64_tr_b16 a[214:215], v236 offset:20992\n"
      "v_mfma_f32_16x16x32_bf16 v[16:19], a[176:179], a[152:155], v[16:19]\n"
      "v_mfma_f32_16x16x32_bf16 v[20:23], a[176:179], a[160:163], v[20:23]\n"
      "ds_read_b64_tr_b16 a[216:217], v237 offset:16384\n"
      "v_mfma_f32_16x16x32_bf16 v[20:23], a[180:183], a[164:167], v[20:23]\n"
      "ds_read_b64_tr_b16 a[218:219], v237 offset:16896\n"
      "v_mfma_f32_16x16x32_bf16 v[24:27], a[184:187], a[144:147], v[24:27]\n"
      "ds_read_b64_tr_b16 a[220:221], v237 offset:20480\n"
      "v_mfma_f32_16x16x32_bf16 v[24:27], a[188:191], a[148:151], v[24:27]\n"
      "ds_read_b64_tr_b16 a[222:223], v237 offset:20992\n"
      "v_mfma_f32_16x16x32_bf16 v[28:31], a[188:191], a[156:159], v[28:31]\n"
      "ds_read_b64_tr_b16 a[224:225], v238 offset:16384\n"
      "v_mfma_f32_16x16x32_bf16 v[28:31], a[184:187], a[152:155], v[28:31]\n"
      "v_mfma_f32_16x16x32_bf16 v[32:35], a[184:187], a[160:163], v[32:35]\n"
      "ds_read_b64_tr_b16 a[226:227], v238 offset:16896\n"
      "v_mfma_f32_16x16x32_bf16 v[32:35], a[188:191], a[164:167], v[32:35]\n"
      "ds_read_b64_tr_b16 a[228:229], v238 offset:20480\n"
      "v_mfma_f32_16x16x32_bf16 v[36:39], a[192:195], a[144:147], v[36:39]\n"
      "ds_read_b64_tr_b16 a[230:231], v238 offset:20992\n"
      "v_mfma_f32_16x16x32_bf16 v[36:39], a[196:199], a[148:151], v[36:39]\n"
      "v_mfma_f32_16x16x32_bf16 v[40:43], a[196:199], a[156:159], v[40:43]\n"
      "v_mfma_f32_16x16x32_bf16 v[40:43], a[192:195], a[152:155], v[40:43]\n"
      "v_mfma_f32_16x16x32_bf16 v[44:47], a[192:195], a[160:163], v[44:47]\n"
      "s_waitcnt vmcnt(10) lgkmcnt(0)\n"
      "v_mfma_f32_16x16x32_bf16 v[44:47], a[196:199], a[164:167], v[44:47]\n"
      "s_barrier\n"
      "v_mfma_f32_16x16x32_bf16 v[48:51], a[200:203], a[144:147], v[48:51]\n"
      "ds_read_b64_tr_b16 a[168:169], v235 offset:32768\n"
      "v_mfma_f32_16x16x32_bf16 v[48:51], a[204:207], a[148:151], v[48:51]\n"
      "s_add_u32 m0, s30, 0\n"
      "ds_read_b64_tr_b16 a[170:171], v235 offset:33280\n"
      "v_mfma_f32_16x16x32_bf16 v[52:55], a[204:207], a[156:159], v[52:55]\n"
      "ds_read_b64_tr_b16 a[172:173], v235 offset:36864\n"
      "v_cvt_pk_bf16_f32 v0, v0, v1\n"
      "v_mfma_f32_16x16x32_bf16 v[52:55], a[200:203], a[152:155], v[52:55]\n"
      "ds_read_b64_tr_b16 a[174:175], v235 offset:37376\n"
      "v_cvt_pk_bf16_f32 v1, v2, v3\n"
      "v_mfma_f32_16x16x32_bf16 v[56:59], a[200:203], a[160:163], v[56:59]\n"
      "ds_read_b64_tr_b16 a[176:177], v236 offset:32768\n"
      "v_cvt_pk_bf16_f32 v2, v12, v13\n"
      "v_mfma_f32_16x16x32_bf16 v[56:59], a[204:207], a[164:167], v[56:59]\n"
      "ds_read_b64_tr_b16 a[178:179], v236 offset:33280\n"
      "v_cvt_pk_bf16_f32 v3, v14, v15\n"
      "v_mfma_f32_16x16x32_bf16 v[60:63], a[208:211], a[144:147], v[60:63]\n"
      "global_load_lds_dwordx4 v227, s[16:17] offset:0 nt\n"
      "v_mfma_f32_16x16x32_bf16 v[60:63], a[212:215], a[148:151], v[60:63]\n"
      "ds_read_b64_tr_b16 a[180:181], v236 offset:36864\n"
      "v_cvt_pk_bf16_f32 v4, v4, v5\n"
      "v_mfma_f32_16x16x32_bf16 v[64:67], a[212:215], a[156:159], v[64:67]\n"
      "ds_read_b64_tr_b16 a[182:183], v236 offset:37376\n"
      "v_cvt_pk_bf16_f32 v5, v6, v7\n"
      "v_mfma_f32_16x16x32_bf16 v[64:67], a[208:211], a[152:155], v[64:67]\n"
      "ds_read_b64_tr_b16 a[184:185], v237 offset:32768\n"
      "v_cvt_pk_bf16_f32 v6, v16, v17\n"
      "v_mfma_f32_16x16x32_bf16 v[68:71], a[208:211], a[160:163], v[68:71]\n"
      "ds_read_b64_tr_b16 a[186:187], v237 offset:33280\n"
      "v_cvt_pk_bf16_f32 v7, v18, v19\n"
      "v_mfma_f32_16x16x32_bf16 v[68:71], a[212:215], a[164:167], v[68:71]\n"
      "ds_read_b64_tr_b16 a[188:189], v237 offset:36864\n"
      "v_cvt_pk_bf16_f32 v8, v8, v9\n"
      "v_mfma_f32_16x16x32_bf16 v[72:75], a[216:219], a[144:147], v[72:75]\n"
      "global_load_lds_dwordx4 v228, s[16:17] offset:1024 nt\n"
      "v_mfma_f32_16x16x32_bf16 v[72:75], a[220:223], a[148:151], v[72:75]\n"
      "ds_read_b64_tr_b16 a[190:191], v237 offset:37376\n"
      "v_cvt_pk_bf16_f32 v9, v10, v11\n"
      "v_mfma_f32_16x16x32_bf16 v[76:79], a[220:223], a[156:159], v[76:79]\n"
      "ds_read_b64_tr_b16 a[192:193], v238 offset:32768\n"
      "v_cvt_pk_bf16_f32 v10, v20, v21\n"
      "v_mfma_f32_16x16x32_bf16 v[76:79], a[216:219], a[152:155], v[76:79]\n"
      "ds_read_b64_tr_b16 a[194:195], v238 offset:33280\n"
      "v_cvt_pk_bf16_f32 v11, v22, v23\n"
      "v_mfma_f32_16x16x32_bf16 v[80:83], a[216:219], a[160:163], v[80:83]\n"
      "ds_read_b64_tr_b16 a[196:197], v238 offset:36864\n"
      "v_cvt_pk_bf16_f32 v24, v24, v25\n"
      "v_mfma_f32_16x16x32_bf16 v[80:83], a[220:223], a[164:167], v[80:83]\n"
      "ds_read_b64_tr_b16 a[198:199], v238 offset:37376\n"
      "v_cvt_pk_bf16_f32 v25, v26, v27\n"
      "v_mfma_f32_16x16x32_bf16 v[84:87], a[224:227], a[144:147], v[84:87]\n"
      "global_load_lds_dwordx4 v229, s[16:17] offset:2048 nt\n"
      "v_mfma_f32_16x16x32_bf16 v[84:87], a[228:231], a[148:151], v[84:87]\n"
      "v_cvt_pk_bf16_f32 v26, v36, v37\n"
      "v_cvt_pk_bf16_f32 v27, v38, v39\n"
      "v_mfma_f32_16x16x32_bf16 v[88:91], a[228:231], a[156:159], v[88:91]\n"
      "v_mfma_f32_16x16x32_bf16 v[88:91], a[224:227], a[152:155], v[88:91]\n"
      "v_mfma_f32_16x16x32_bf16 v[92:95], a[224:227], a[160:163], v[92:95]\n"
      "s_waitcnt vmcnt(3) lgkmcnt(0)\n"
      "v_mfma_f32_16x16x32_bf16 v[92:95], a[228:231], a[164:167], v[92:95]\n"
      "s_barrier\n"
      "v_mfma_f32_16x16x32_bf16 v[96:99], a[168:171], a[144:147], v[96:99]\n"
      "ds_read_b64_tr_b16 a[200:201], v235 offset:49152\n"
      "v_mfma_f32_16x16x32_bf16 v[96:99], a[172:175], a[148:151], v[96:99]\n"
      "ds_read_b64_tr_b16 a[202:203], v235 offset:49664\n"
      "v_cvt_pk_bf16_f32 v28, v28, v29\n"
      "v_mfma_f32_16x16x32_bf16 v[100:103], a[172:175], a[156:159], v[100:103]\n"
      "ds_read_b64_tr_b16 a[204:205], v235 offset:53248\n"
      "v_cvt_pk_bf16_f32 v29, v30, v31\n"
      "v_mfma_f32_16x16x32_bf16 v[100:103], a[168:171], a[152:155], v[100:103]\n"
      "global_load_lds_dwordx4 v230, s[16:17] offset:3072 nt\n"
      "v_mfma_f32_16x16x32_bf16 v[104:107], a[168:171], a[160:163], v[104:107]\n"
      "ds_read_b64_tr_b16 a[206:207], v235 offset:53760\n"
      "v_cvt_pk_bf16_f32 v30, v40, v41\n"
      "v_mfma_f32_16x16x32_bf16 v[104:107], a[172:175], a[164:167], v[104:107]\n"
      "ds_read_b64_tr_b16 a[208:209], v236 offset:49152\n"
      "v_cvt_pk_bf16_f32 v31, v42, v43\n"
      "v_mfma_f32_16x16x32_bf16 v[108:111], a[176:179], a[144:147], v[108:111]\n"
      "ds_read_b64_tr_b16 a[210:211], v236 offset:49664\n"
      "v_cvt_pk_bf16_f32 v32, v32, v33\n"
      "v_mfma_f32_16x16x32_bf16 v[108:111], a[180:183], a[148:151], v[108:111]\n"
      "ds_read_b64_tr_b16 a[212:213], v236 offset:53248\n"
      "v_cvt_pk_bf16_f32 v33, v34, v35\n"
      "v_mfma_f32_16x16x32_bf16 v[112:115], a[180:183], a[156:159], v[112:115]\n"
      "ds_read_b64_tr_b16 a[214:215], v236 offset:53760\n"
      "v_cvt_pk_bf16_f32 v34, v44, v45\n"
      "v_mfma_f32_16x16x32_bf16 v[112:115], a[176:179], a[152:155], v[112:115]\n"
      "s_add_u32 s16, s16, 16384\n"
      "s_addc_u32 s17, s17, 0\n"
      "v_mfma_f32_16x16x32_bf16 v[116:119], a[176:179], a[160:163], v[116:119]\n"
      "ds_read_b64_tr_b16 a[216:217], v237 offset:49152\n"
      "v_cvt_pk_bf16_f32 v35, v46, v47\n"
      "v_mfma_f32_16x16x32_bf16 v[116:119], a[180:183], a[164:167], v[116:119]\n"
      "ds_read_b64_tr_b16 a[218:219], v237 offset:49664\n"
      "v_cvt_pk_bf16_f32 v48, v48, v49\n"
      "v_mfma_f32_16x16x32_bf16 v[120:123], a[184:187], a[144:147], v[120:123]\n"
      "ds_read_b64_tr_b16 a[220:221], v237 offset:53248\n"
      "v_cvt_pk_bf16_f32 v49, v50, v51\n"
      "v_mfma_f32_16x16x32_bf16 v[120:123], a[188:191], a[148:151], v[120:123]\n"
      "ds_read_b64_tr_b16 a[222:223], v237 offset:53760\n"
      "v_cvt_pk_bf16_f32 v50, v60, v61\n"
      "v_mfma_f32_16x16x32_bf16 v[124:127], a[188:191], a[156:159], v[124:127]\n"
      "s_add_u32 m0, s30, 4096\n"
      "ds_read_b64_tr_b16 a[224:225], v238 offset:49152\n"
      "v_mfma_f32_16x16x32_bf16 v[124:127], a[184:187], a[152:155], v[124:127]\n"
      "ds_read_b64_tr_b16 a[226:227], v238 offset:49664\n"
      "v_cvt_pk_bf16_f32 v51, v62, v63\n"
      "v_mfma_f32_16x16x32_bf16 v[128:131], a[184:187], a[160:163], v[128:131]\n"
      "ds_read_b64_tr_b16 a[228:229], v238 offset:53248\n"
      "v_cvt_pk_bf16_f32 v52, v52, v53\n"
      "v_mfma_f32_16x16x32_bf16 v[128:131], a[188:191], a[164:167], v[128:131]\n"
      "ds_read_b64_tr_b16 a[230:231], v238 offset:53760\n"
      "v_cvt_pk_bf16_f32 v53, v54, v55\n"
      "v_mfma_f32_16x16x32_bf16 v[132:135], a[192:195], a[144:147], v[132:135]\n"
      "global_load_lds_dwordx4 v227, s[16:17] offset:0 nt\n"
      "v_mfma_f32_16x16x32_bf16 v[132:135], a[196:199], a[148:151], v[132:135]\n"
      "v_cvt_pk_bf16_f32 v54, v64, v65\n"
      "v_cvt_pk_bf16_f32 v55, v66, v67\n"
      "v_mfma_f32_16x16x32_bf16 v[136:139], a[196:199], a[156:159], v[136:139]\n"
      "v_mfma_f32_16x16x32_bf16 v[136:139], a[192:195], a[152:155], v[136:139]\n"
      "v_mfma_f32_16x16x32_bf16 v[140:143], a[192:195], a[160:163], v[140:143]\n"
      "s_waitcnt lgkmcnt(0)\n"
      "v_mfma_f32_16x16x32_bf16 v[140:143], a[196:199], a[164:167], v[140:143]\n"
      "s_barrier\n"
      "v_mfma_f32_16x16x32_bf16 v[144:147], a[200:203], a[144:147], v[144:147]\n"
      "ds_read_b64 a[0:1], v241 offset:0\n"
      "v_mfma_f32_16x16x32_bf16 v[144:147], a[204:207], a[148:151], v[144:147]\n"
      "ds_read_b64 a[2:3], v242 offset:0\n"
      "ds_read_b64 a[4:5], v243 offset:0\n"
      "v_mfma_f32_16x16x32_bf16 v[148:151], a[204:207], a[156:159], v[148:151]\n"
      "ds_read_b64 a[6:7], v244 offset:0\n"
      "v_cvt_pk_bf16_f32 v56, v56, v57\n"
      "v_mfma_f32_16x16x32_bf16 v[148:151], a[200:203], a[152:155], v[148:151]\n"
      "global_load_lds_dwordx4 v228, s[16:17] offset:1024 nt\n"
      "v_mfma_f32_16x16x32_bf16 v[152:155], a[200:203], a[160:163], v[152:155]\n"
      "ds_read_b64 a[8:9], v241 offset:2048\n"
      "v_cvt_pk_bf16_f32 v57, v58, v59\n"
      "v_mfma_f32_16x16x32_bf16 v[152:155], a[204:207], a[164:167], v[152:155]\n"
      "ds_read_b64 a[10:11], v242 offset:2048\n"
      "ds_read_b64 a[12:13], v243 offset:2048\n"
      "v_mfma_f32_16x16x32_bf16 v[156:159], a[208:211], a[144:147], v[156:159]\n"
      "ds_read_b64 a[14:15], v244 offset:2048\n"
      "v_cvt_pk_bf16_f32 v58, v68, v69\n"
      "v_mfma_f32_16x16x32_bf16 v[156:159], a[212:215], a[148:151], v[156:159]\n"
      "ds_read_b64 a[16:17], v241 offset:4096\n"
      "v_cvt_pk_bf16_f32 v59, v70, v71\n"
      "v_mfma_f32_16x16x32_bf16 v[160:163], a[212:215], a[156:159], v[160:163]\n"
      "ds_read_b64 a[18:19], v242 offset:4096\n"
      "ds_read_b64 a[20:21], v243 offset:4096\n"
      "v_mfma_f32_16x16x32_bf16 v[160:163], a[208:211], a[152:155], v[160:163]\n"
      "s_add_u32 s16, s10, s19\n"
      "s_addc_u32 s17, s11, 0\n"
      "v_mfma_f32_16x16x32_bf16 v[164:167], a[208:211], a[160:163], v[164:167]\n"
      "ds_read_b64 a[22:23], v244 offset:4096\n"
      "v_cvt_pk_bf16_f32 v72, v72, v73\n"
      "v_mfma_f32_16x16x32_bf16 v[164:167], a[212:215], a[164:167], v[164:167]\n"
      "ds_read_b64 a[24:25], v241 offset:6144\n"
      "ds_read_b64 a[26:27], v242 offset:6144\n"
      "v_mfma_f32_16x16x32_bf16 v[168:171], a[216:219], a[144:147], v[168:171]\n"
      "ds_read_b64 a[28:29], v243 offset:6144\n"
      "v_cvt_pk_bf16_f32 v73, v74, v75\n"
      "v_mfma_f32_16x16x32_bf16 v[168:171], a[220:223], a[148:151], v[168:171]\n"
      "ds_read_b64 a[30:31], v244 offset:6144\n"
      "v_cvt_pk_bf16_f32 v74, v84, v85\n"
      "v_mfma_f32_16x16x32_bf16 v[172:175], a[220:223], a[156:159], v[172:175]\n"
      "s_add_u32 m0, s30, 24576\n"
      "ds_read_b64 a[32:33], v241 offset:8192\n"
      "v_mfma_f32_16x16x32_bf16 v[172:175], a[216:219], a[152:155], v[172:175]\n"
      "ds_read_b64 a[34:35], v242 offset:8192\n"
      "ds_read_b64 a[36:37], v243 offset:8192\n"
      "v_mfma_f32_16x16x32_bf16 v[176:179], a[216:219], a[160:163], v[176:179]\n"
      "ds_read_b64 a[38:39], v244 offset:8192\n"
      "v_cvt_pk_bf16_f32 v75, v86, v87\n"
      "v_mfma_f32_16x16x32_bf16 v[176:179], a[220:223], a[164:167], v[176:179]\n"
      "ds_read_b64 a[40:41], v241 offset:10240\n"
      "v_cvt_pk_bf16_f32 v76, v76, v77\n"
      "v_mfma_f32_16x16x32_bf16 v[180:183], a[224:227], a[144:147], v[180:183]\n"
      "global_load_lds_dwordx4 v227, s[16:17] offset:0 nt\n"
      "v_mfma_f32_16x16x32_bf16 v[180:183], a[228:231], a[148:151], v[180:183]\n"
      "ds_read_b64 a[42:43], v242 offset:10240\n"
      "ds_read_b64 a[44:45], v243 offset:10240\n"
      "v_mfma_f32_16x16x32_bf16 v[184:187], a[228:231], a[156:159], v[184:187]\n"
      "ds_read_b64 a[46:47], v244 offset:10240\n"
      "v_cvt_pk_bf16_f32 v77, v78, v79\n"
      "v_mfma_f32_16x16x32_bf16 v[184:187], a[224:227], a[152:155], v[184:187]\n"
      "v_cvt_pk_bf16_f32 v78, v88, v89\n"
      "v_cvt_pk_bf16_f32 v79, v90, v91\n"
      "v_mfma_f32_16x16x32_bf16 v[188:191], a[224:227], a[160:163], v[188:191]\n"
      "v_cvt_pk_bf16_f32 v80, v80, v81\n"
      "s_waitcnt lgkmcnt(0)\n"
      "v_mfma_f32_16x16x32_bf16 v[188:191], a[228:231], a[164:167], v[188:191]\n"
      "v_cvt_pk_bf16_f32 v81, v82, v83\n"
      "v_cvt_pk_bf16_f32 v82, v92, v93\n"
      "v_cvt_pk_bf16_f32 v83, v94, v95\n"
      "s_barrier\n"
      ".Lfa_join:\n"
      "v_mfma_f32_16x16x32_bf16 v[192:195], a[0:3], v[0:3], 0\n"
      "ds_read_b64 a[48:49], v241 offset:24576\n"
      "v_mfma_f32_16x16x32_bf16 v[192:195], a[4:7], v[24:27], v[192:195]\n"
      "ds_read_b64 a[50:51], v242 offset:24576\n"
      "v_mfma_f32_16x16x32_bf16 v[196:199], a[4:7], v[28:31], 0\n"
      "ds_read_b64 a[52:53], v243 offset:24576\n"
      "v_mfma_f32_16x16x32_bf16 v[196:199], a[0:3], v[4:7], v[196:199]\n"
      "v_add_u32 v254, 1, v254\n"
      "ds_read_b64 a[54:55], v244 offset:24576\n"
      "v_mfma_f32_16x16x32_bf16 v[200:203], a[0:3], v[8:11], 0\n"
      "v_cvt_pk_bf16_f32 v96, v96, v97\n"
      "v_cvt_pk_bf16_f32 v97, v98, v99\n"
      "v_mfma_f32_16x16x32_bf16 v[200:203], a[4:7], v[32:35], v[200:203]\n"
      "ds_read_b64 a[56:57], v241 offset:26624\n"
      "v_cvt_pk_bf16_f32 v98, v108, v109\n"
      "v_mfma_f32_16x16x32_bf16 v[204:207], a[8:11], v[0:3], 0\n"
      "ds_read_b64 a[58:59], v242 offset:26624\n"
      "v_cvt_pk_bf16_f32 v99, v110, v111\n"
      "v_mfma_f32_16x16x32_bf16 v[204:207], a[12:15], v[24:27], v[204:207]\n"
      "global_load_lds_dwordx4 v228, s[16:17] offset:1024 nt\n"
      "v_mfma_f32_16x16x32_bf16 v[208:211], a[12:15], v[28:31], 0\n"
      "ds_read_b64 a[60:61], v243 offset:26624\n"
      "v_cvt_pk_bf16_f32 v100, v100, v101\n"
      "v_mfma_f32_16x16x32_bf16 v[208:211], a[8:11], v[4:7], v[208:211]\n"
      "ds_read_b64 a[62:63], v244 offset:26624\n"
      "v_cvt_pk_bf16_f32 v101, v102, v103\n"
      "v_mfma_f32_16x16x32_bf16 v[212:215], a[8:11], v[8:11], 0\n"
      "ds_read_b64 a[64:65], v241 offset:28672\n"
      "v_cvt_pk_bf16_f32 v102, v112, v113\n"
      "v_mfma_f32_16x16x32_bf16 v[212:215], a[12:15], v[32:35], v[212:215]\n"
      "ds_read_b64 a[66:67], v242 offset:28672\n"
      "v_cvt_pk_bf16_f32 v103, v114, v115\n"
      "v_mfma_f32_16x16x32_bf16 v[12:15], a[16:19], v[0:3], 0\n"
      "ds_read_b64 a[68:69], v243 offset:28672\n"
      "v_cvt_pk_bf16_f32 v104, v104, v105\n"
      "v_mfma_f32_16x16x32_bf16 v[12:15], a[20:23], v[24:27], v[12:15]\n"
      "global_load_lds_dwordx4 v229, s[16:17] offset:2048 nt\n"
      "v_mfma_f32_16x16x32_bf16 v[16:19], a[20:23], v[28:31], 0\n"
      "ds_read_b64 a[70:71], v244 offset:28672\n"
      "v_cvt_pk_bf16_f32 v105, v106, v107\n"
      "v_mfma_f32_16x16x32_bf16 v[16:19], a[16:19], v[4:7], v[16:19]\n"
      "v_cvt_pk_bf16_f32 v106, v116, v117\n"
      "v_cvt_pk_bf16_f32 v107, v118, v119\n"
      "v_mfma_f32_16x16x32_bf16 v[20:23], a[16:19], v[8:11], 0\n"
      "ds_read_b64 a[72:73], v241 offset:30720\n"
      "v_cvt_pk_bf16_f32 v120, v120, v121\n"
      "v_mfma_f32_16x16x32_bf16 v[20:23], a[20:23], v[32:35], v[20:23]\n"
      "ds_read_b64 a[74:75], v242 offset:30720\n"
      "v_cvt_pk_bf16_f32 v121, v122, v123\n"
      "v_mfma_f32_16x16x32_bf16 v[36:39], a[24:27], v[0:3], 0\n"
      "global_load_lds_dwordx4 v230, s[16:17] offset:3072 nt\n"
      "v_mfma_f32_16x16x32_bf16 v[36:39], a[28:31], v[24:27], v[36:39]\n"
      "ds_read_b64 a[76:77], v243 offset:30720\n"
      "v_cvt_pk_bf16_f32 v122, v132, v133\n"
      "v_mfma_f32_16x16x32_bf16 v[40:43], a[28:31], v[28:31], 0\n"
      "ds_read_b64 a[78:79], v244 offset:30720\n"
      "v_cvt_pk_bf16_f32 v123, v134, v135\n"
      "v_mfma_f32_16x16x32_bf16 v[40:43], a[24:27], v[4:7], v[40:43]\n"
      "ds_read_b64 a[80:81], v241 offset:32768\n"
      "v_cvt_pk_bf16_f32 v124, v124, v125\n"
      "v_mfma_f32_16x16x32_bf16 v[44:47], a[24:27], v[8:11], 0\n"
      "ds_read_b64 a[82:83], v242 offset:32768\n"
      "v_cvt_pk_bf16_f32 v125, v126, v127\n"
      "v_mfma_f32_16x16x32_bf16 v[44:47], a[28:31], v[32:35], v[44:47]\n"
      "s_add_u32 s16, s16, 16384\n"
      "s_addc_u32 s17, s17, 0\n"
      "v_mfma_f32_16x16x32_bf16 v[60:63], a[32:35], v[0:3], 0\n"
      "ds_read_b64 a[84:85], v243 offset:32768\n"
      "v_cvt_pk_bf16_f32 v126, v136, v137\n"
      "v_mfma_f32_16x16x32_bf16 v[60:63], a[36:39], v[24:27], v[60:63]\n"
      "ds_read_b64 a[86:87], v244 offset:32768\n"
      "v_cvt_pk_bf16_f32 v127, v138, v139\n"
      "v_mfma_f32_16x16x32_bf16 v[64:67], a[36:39], v[28:31], 0\n"
      "v_cvt_pk_bf16_f32 v128, v128, v129\n"
      "v_cvt_pk_bf16_f32 v129, v130, v131\n"
      "v_mfma_f32_16x16x32_bf16 v[64:67], a[32:35], v[4:7], v[64:67]\n"
      "s_add_u32 m0, s30, 28672\n"
      "ds_read_b64 a[88:89], v241 offset:34816\n"
      "v_mfma_f32_16x16x32_bf16 v[68:71], a[32:35], v[8:11], 0\n"
      "ds_read_b64 a[90:91], v242 offset:34816\n"
      "v_cvt_pk_bf16_f32 v130, v140, v141\n"
      "v_mfma_f32_16x16x32_bf16 v[68:71], a[36:39], v[32:35], v[68:71]\n"
      "ds_read_b64 a[92:93], v243 offset:34816\n"
      "v_cvt_pk_bf16_f32 v131, v142, v143\n"
      "v_mfma_f32_16x16x32_bf16 v[84:87], a[40:43], v[0:3], 0\n"
      "ds_read_b64 a[94:95], v244 offset:34816\n"
      "v_mfma_f32_16x16x32_bf16 v[84:87], a[44:47], v[24:27], v[84:87]\n"
      "global_load_lds_dwordx4 v227, s[16:17] offset:0 nt\n"
      "v_mfma_f32_16x16x32_bf16 v[88:91], a[44:47], v[28:31], 0\n"
      "v_mfma_f32_16x16x32_bf16 v[88:91], a[40:43], v[4:7], v[88:91]\n"
      "v_mfma_f32_16x16x32_bf16 v[92:95], a[40:43], v[8:11], 0\n"
      "s_waitcnt lgkmcnt(0)\n"
      "v_mfma_f32_16x16x32_bf16 v[92:95], a[44:47], v[32:35], v[92:95]\n"
      "s_barrier\n"
      "v_mfma_f32_16x16x32_bf16 v[192:195], a[48:51], v[48:51], v[192:195]\n"
      "v_add_u32 v241, 49152, v241\n"
      "v_mfma_f32_16x16x32_bf16 v[192:195], a[52:55], v[72:75], v[192:195]\n"
      "v_add_u32 v242, 49152, v242\n"
      "v_mfma_f32_16x16x32_bf16 v[196:199], a[52:55], v[76:79], v[196:199]\n"
      "v_add_u32 v243, 49152, v243\n"
      "v_mfma_f32_16x16x32_bf16 v[196:199], a[48:51], v[52:55], v[196:199]\n"
      "global_load_lds_dwordx4 v228, s[16:17] offset:1024 nt\n"
      "v_mfma_f32_16x16x32_bf16 v[200:203], a[48:51], v[56:59], v[200:203]\n"
      "v_add_u32 v244, 49152, v244\n"
      "v_cvt_pk_bf16_f32 v144, v144, v145\n"
      "v_mfma_f32_16x16x32_bf16 v[200:203], a[52:55], v[80:83], v[200:203]\n"
      "ds_read_b64 a[0:1], v241 offset:0\n"
      "v_cvt_pk_bf16_f32 v145, v146, v147\n"
      "v_mfma_f32_16x16x32_bf16 v[204:207], a[56:59], v[48:51], v[204:207]\n"
      "ds_read_b64 a[2:3], v242 offset:0\n"
      "v_cvt_pk_bf16_f32 v146, v156, v157\n"
      "v_mfma_f32_16x16x32_bf16 v[204:207], a[60:63], v[72:75], v[204:207]\n"
      "ds_read_b64 a[4:5], v243 offset:0\n"
      "v_cvt_pk_bf16_f32 v147, v158, v159\n"
      "v_mfma_f32_16x16x32_bf16 v[208:211], a[60:63], v[76:79], v[208:211]\n"
      "ds_read_b64 a[6:7], v244 offset:0\n"
      "v_cvt_pk_bf16_f32 v148, v148, v149\n"
      "v_mfma_f32_16x16x32_bf16 v[208:211], a[56:59], v[52:55], v[208:211]\n"
      "ds_read_b64 a[8:9], v241 offset:2048\n"
      "v_cvt_pk_bf16_f32 v149, v150, v151\n"
      "v_mfma_f32_16x16x32_bf16 v[212:215], a[56:59], v[56:59], v[212:215]\n"
      "ds_read_b64 a[10:11], v242 offset:2048\n"
      "v_cvt_pk_bf16_f32 v150, v160, v161\n"
      "v_mfma_f32_16x16x32_bf16 v[212:215], a[60:63], v[80:83], v[212:215]\n"
      "s_add_u32 s16, s10, s20\n"
      "s_addc_u32 s17, s11, 0\n"
      "v_mfma_f32_16x16x32_bf16 v[12:15], a[64:67], v[48:51], v[12:15]\n"
      "ds_read_b64 a[12:13], v243 offset:2048\n"
      "v_cvt_pk_bf16_f32 v151, v162, v163\n"
      "v_mfma_f32_16x16x32_bf16 v[12:15], a[68:71], v[72:75], v[12:15]\n"
      "ds_read_b64 a[14:15], v244 offset:2048\n"
      "v_cvt_pk_bf16_f32 v152, v152, v153\n"
      "v_mfma_f32_16x16x32_bf16 v[16:19], a[68:71], v[76:79], v[16:19]\n"
      "ds_read_b64 a[16:17], v241 offset:4096\n"
      "v_cvt_pk_bf16_f32 v153, v154, v155\n"
      "v_mfma_f32_16x16x32_bf16 v[16:19], a[64:67], v[52:55], v[16:19]\n"
      "ds_read_b64 a[18:19], v242 offset:4096\n"
      "ds_read_b64 a[20:21], v243 offset:4096\n"
      "v_mfma_f32_16x16x32_bf16 v[20:23], a[64:67], v[56:59], v[20:23]\n"
      "s_add_u32 m0, s30, 65536\n"
      "ds_read_b64 a[22:23], v244 offset:4096\n"
      "v_mfma_f32_16x16x32_bf16 v[20:23], a[68:71], v[80:83], v[20:23]\n"
      "ds_read_b64 a[24:25], v241 offset:6144\n"
      "v_cvt_pk_bf16_f32 v154, v164, v165\n"
      "v_mfma_f32_16x16x32_bf16 v[36:39], a[72:75], v[48:51], v[36:39]\n"
      "ds_read_b64 a[26:27], v242 offset:6144\n"
      "v_cvt_pk_bf16_f32 v155, v166, v167\n"
      "v_mfma_f32_16x16x32_bf16 v[36:39], a[76:79], v[72:75], v[36:39]\n"
      "ds_read_b64 a[28:29], v243 offset:6144\n"
      "v_cvt_pk_bf16_f32 v168, v168, v169\n"
      "v_mfma_f32_16x16x32_bf16 v[40:43], a[76:79], v[76:79], v[40:43]\n"
      "ds_read_b64 a[30:31], v244 offset:6144\n"
      "v_cvt_pk_bf16_f32 v169, v170, v171\n"
      "v_mfma_f32_16x16x32_bf16 v[40:43], a[72:75], v[52:55], v[40:43]\n"
      "ds_read_b64 a[32:33], v241 offset:8192\n"
      "v_cvt_pk_bf16_f32 v170, v180, v181\n"
      "v_mfma_f32_16x16x32_bf16 v[44:47], a[72:75], v[56:59], v[44:47]\n"
      "global_load_lds_dwordx4 v227, s[16:17] offset:0 nt\n"
      "v_mfma_f32_16x16x32_bf16 v[44:47], a[76:79], v[80:83], v[44:47]\n"
      "ds_read_b64 a[34:35], v242 offset:8192\n"
      "v_cvt_pk_bf16_f32 v171, v182, v183\n"
      "v_mfma_f32_16x16x32_bf16 v[60:63], a[80:83], v[48:51], v[60:63]\n"
      "ds_read_b64 a[36:37], v243 offset:8192\n"
      "v_cvt_pk_bf16_f32 v172, v172, v173\n"
      "v_mfma_f32_16x16x32_bf16 v[60:63], a[84:87], v[72:75], v[60:63]\n"
      "ds_read_b64 a[38:39], v244 offset:8192\n"
      "v_cvt_pk_bf16_f32 v173, v174, v175\n"
      "v_mfma_f32_16x16x32_bf16 v[64:67], a[84:87], v[76:79], v[64:67]\n"
      "ds_read_b64 a[40:41], v241 offset:10240\n"
      "v_cvt_pk_bf16_f32 v174, v184, v185\n"
      "v_mfma_f32_16x16x32_bf16 v[64:67], a[80:83], v[52:55], v[64:67]\n"
      "ds_read_b64 a[42:43], v242 offset:10240\n"
      "v_cvt_pk_bf16_f32 v175, v186, v187\n"
      "v_mfma_f32_16x16x32_bf16 v[68:71], a[80:83], v[56:59], v[68:71]\n"
      "ds_read_b64 a[44:45], v243 offset:10240\n"
      "v_cvt_pk_bf16_f32 v176, v176, v177\n"
      "v_mfma_f32_16x16x32_bf16 v[68:71], a[84:87], v[80:83], v[68:71]\n"
      "ds_read_b64 a[46:47], v244 offset:10240\n"
      "v_cvt_pk_bf16_f32 v177, v178, v179\n"
      "v_mfma_f32_16x16x32_bf16 v[84:87], a[88:91], v[48:51], v[84:87]\n"
      "global_load_lds_dwordx4 v228, s[16:17] offset:1024 nt\n"
      "v_mfma_f32_16x16x32_bf16 v[84:87], a[92:95], v[72:75], v[84:87]\n"
      "v_cvt_pk_bf16_f32 v178, v188, v189\n"
      "v_cvt_pk_bf16_f32 v179, v190, v191\n"
      "v_mfma_f32_16x16x32_bf16 v[88:91], a[92:95], v[76:79], v[88:91]\n"
      "v_mfma_f32_16x16x32_bf16 v[88:91], a[88:91], v[52:55], v[88:91]\n"
      "v_mfma_f32_16x16x32_bf16 v[92:95], a[88:91], v[56:59], v[92:95]\n"
      "s_waitcnt lgkmcnt(0)\n"
      "v_mfma_f32_16x16x32_bf16 v[92:95], a[92:95], v[80:83], v[92:95]\n"
      "s_barrier\n"
      "v_mfma_f32_16x16x32_bf16 v[192:195], a[0:3], v[96:99], v[192:195]\n"
      "ds_read_b64 a[48:49], v241 offset:24576\n"
      "v_mfma_f32_16x16x32_bf16 v[192:195], a[4:7], v[120:123], v[192:195]\n"
      "ds_read_b64 a[50:51], v242 offset:24576\n"
      "v_mfma_f32_16x16x32_bf16 v[196:199], a[4:7], v[124:127], v[196:199]\n"
      "global_load_lds_dwordx4 v229, s[16:17] offset:2048 nt\n"
      "v_mfma_f32_16x16x32_bf16 v[196:199], a[0:3], v[100:103], v[196:199]\n"
      "ds_read_b64 a[52:53], v243 offset:24576\n"
      "v_mfma_f32_16x16x32_bf16 v[200:203], a[0:3], v[104:107], v[200:203]\n"
      "ds_read_b64 a[54:55], v244 offset:24576\n"
      "v_xor_b32 v223, 0x2000, v249\n"
      "v_mfma_f32_16x16x32_bf16 v[200:203], a[4:7], v[128:131], v[200:203]\n"
      "ds_read_b64 a[56:57], v241 offset:26624\n"
      "v_xor_b32 v224, 0x2000, v250\n"
      "v_mfma_f32_16x16x32_bf16 v[204:207], a[8:11], v[96:99], v[204:207]\n"
      "ds_read_b64 a[58:59], v242 offset:26624\n"
      "v_add_u32 v216, 65536, v249\n"
      "v_mfma_f32_16x16x32_bf16 v[204:207], a[12:15], v[120:123], v[204:207]\n"
      "global_load_lds_dwordx4 v230, s[16:17] offset:3072 nt\n"
      "v_mfma_f32_16x16x32_bf16 v[208:211], a[12:15], v[124:127], v[208:211]\n"
      "v_add_u32 v217, 65536, v250\n"
      "v_add_u32 v218, 65536, v223\n"
      "v_mfma_f32_16x16x32_bf16 v[208:211], a[8:11], v[100:103], v[208:211]\n"
      "ds_read_b64 a[60:61], v243 offset:26624\n"
      "v_add_u32 v219, 65536, v224\n"
      "v_mfma_f32_16x16x32_bf16 v[212:215], a[8:11], v[104:107], v[212:215]\n"
      "ds_read_b64 a[62:63], v244 offset:26624\n"
      "v_add_u32 v235, 131072, v249\n"
      "v_mfma_f32_16x16x32_bf16 v[212:215], a[12:15], v[128:131], v[212:215]\n"
      "ds_read_b64 a[64:65], v241 offset:28672\n"
      "v_add_u32 v236, 131072, v250\n"
      "v_mfma_f32_16x16x32_bf16 v[12:15], a[16:19], v[96:99], v[12:15]\n"
      "ds_read_b64 a[66:67], v242 offset:28672\n"
      "v_add_u32 v237, 131072, v223\n"
      "v_mfma_f32_16x16x32_bf16 v[12:15], a[20:23], v[120:123], v[12:15]\n"
      "s_add_u32 s16, s16, 16384\n"
      "s_addc_u32 s17, s17, 0\n"
      "v_mfma_f32_16x16x32_bf16 v[16:19], a[20:23], v[124:127], v[16:19]\n"
      "ds_read_b64 a[68:69], v243 offset:28672\n"
      "v_add_u32 v238, 131072, v224\n"
      "v_mfma_f32_16x16x32_bf16 v[16:19], a[16:19], v[100:103], v[16:19]\n"
      "ds_read_b64 a[70:71], v244 offset:28672\n"
      "v_mfma_f32_16x16x32_bf16 v[20:23], a[16:19], v[104:107], v[20:23]\n"
      "s_add_u32 m0, s30, 69632\n"
      "ds_read_b64 a[72:73], v241 offset:30720\n"
      "v_mfma_f32_16x16x32_bf16 v[20:23], a[20:23], v[128:131], v[20:23]\n"
      "ds_read_b64 a[74:75], v242 offset:30720\n"
      "v_mfma_f32_16x16x32_bf16 v[36:39], a[24:27], v[96:99], v[36:39]\n"
      "ds_read_b64 a[76:77], v243 offset:30720\n"
      "v_mfma_f32_16x16x32_bf16 v[36:39], a[28:31], v[120:123], v[36:39]\n"
      "ds_read_b64 a[78:79], v244 offset:30720\n"
      "v_mfma_f32_16x16x32_bf16 v[40:43], a[28:31], v[124:127], v[40:43]\n"
      "global_load_lds_dwordx4 v227, s[16:17] offset:0 nt\n"
      "v_mfma_f32_16x16x32_bf16 v[40:43], a[24:27], v[100:103], v[40:43]\n"
      "ds_read_b64 a[80:81], v241 offset:32768\n"
      "v_mfma_f32_16x16x32_bf16 v[44:47], a[24:27], v[104:107], v[44:47]\n"
      "ds_read_b64 a[82:83], v242 offset:32768\n"
      "v_mfma_f32_16x16x32_bf16 v[44:47], a[28:31], v[128:131], v[44:47]\n"
      "v_mfma_f32_16x16x32_bf16 v[60:63], a[32:35], v[96:99], v[60:63]\n"
      "ds_read_b64 a[84:85], v243 offset:32768\n"
      "v_mfma_f32_16x16x32_bf16 v[60:63], a[36:39], v[120:123], v[60:63]\n"
      "ds_read_b64 a[86:87], v244 offset:32768\n"
      "v_mfma_f32_16x16x32_bf16 v[64:67], a[36:39], v[124:127], v[64:67]\n"
      "global_load_lds_dwordx4 v228, s[16:17] offset:1024 nt\n"
      "v_mfma_f32_16x16x32_bf16 v[64:67], a[32:35], v[100:103], v[64:67]\n"
      "ds_read_b64 a[88:89], v241 offset:34816\n"
      "v_mfma_f32_16x16x32_bf16 v[68:71], a[32:35], v[104:107], v[68:71]\n"
      "ds_read_b64 a[90:91], v242 offset:34816\n"
      "v_mfma_f32_16x16x32_bf16 v[68:71], a[36:39], v[128:131], v[68:71]\n"
      "ds_read_b64 a[92:93], v243 offset:34816\n"
      "v_mfma_f32_16x16x32_bf16 v[84:87], a[40:43], v[96:99], v[84:87]\n"
      "ds_read_b64 a[94:95], v244 offset:34816\n"
      "v_mfma_f32_16x16x32_bf16 v[84:87], a[44:47], v[120:123], v[84:87]\n"
      "s_add_u32 s16, s10, s21\n"
      "s_addc_u32 s17, s11, 0\n"
      "v_mfma_f32_16x16x32_bf16 v[88:91], a[44:47], v[124:127], v[88:91]\n"
      "v_mfma_f32_16x16x32_bf16 v[88:91], a[40:43], v[100:103], v[88:91]\n"
      "v_mfma_f32_16x16x32_bf16 v[92:95], a[40:43], v[104:107], v[92:95]\n"
      "s_waitcnt lgkmcnt(0)\n"
      "v_mfma_f32_16x16x32_bf16 v[92:95], a[44:47], v[128:131], v[92:95]\n"
      "s_barrier\n"
      "v_mfma_f32_16x16x32_bf16 v[192:195], a[48:51], v[144:147], v[192:195]\n"
      "s_cmp_eq_u32 s46, 7\n"
      "s_cselect_b32 s74, 6, 7\n"
      "v_mfma_f32_16x16x32_bf16 v[192:195], a[52:55], v[168:171], v[192:195]\n"
      "v_readlane_b32 s47, v254, s74\n"
      "v_readlane_b32 s52, v220, 0\n"
      "v_mfma_f32_16x16x32_bf16 v[196:199], a[52:55], v[172:175], v[196:199]\n"
      "s_add_u32 m0, s30, 90112\n"
      "v_readlane_b32 s59, v254, 0\n"
      "v_mfma_f32_16x16x32_bf16 v[196:199], a[48:51], v[148:151], v[196:199]\n"
      "v_readlane_b32 s53, v220, 1\n"
      "v_readlane_b32 s60, v254, 1\n"
      "v_mfma_f32_16x16x32_bf16 v[200:203], a[48:51], v[152:155], v[200:203]\n"
      "v_readlane_b32 s54, v220, 2\n"
      "v_readlane_b32 s61, v254, 2\n"
      "v_mfma_f32_16x16x32_bf16 v[200:203], a[52:55], v[176:179], v[200:203]\n"
      "v_readlane_b32 s55, v220, 3\n"
      "v_readlane_b32 s62, v254, 3\n"
      "v_mfma_f32_16x16x32_bf16 v[204:207], a[56:59], v[144:147], v[204:207]\n"
      "v_readlane_b32 s56, v220, 4\n"
      "v_readlane_b32 s63, v254, 4\n"
      "v_mfma_f32_16x16x32_bf16 v[204:207], a[60:63], v[168:171], v[204:207]\n"
      "v_readlane_b32 s57, v220, 5\n"
      "v_readlane_b32 s64, v254, 5\n"
      "v_mfma_f32_16x16x32_bf16 v[208:211], a[60:63], v[172:175], v[208:211]\n"
      "v_readlane_b32 s58, v220, 6\n"
      "v_readlane_b32 s65, v254, 6\n"
      "v_mfma_f32_16x16x32_bf16 v[208:211], a[56:59], v[148:151], v[208:211]\n"
      "v_subrev_u32 v220, 25165824, v255\n"
      "v_lshrrev_b32 v220, 1, v220\n"
      "v_mfma_f32_16x16x32_bf16 v[212:215], a[56:59], v[152:155], v[212:215]\n"
      "global_load_lds_dwordx4 v227, s[16:17] offset:0 nt\n"
      "v_mfma_f32_16x16x32_bf16 v[212:215], a[60:63], v[176:179], v[212:215]\n"
      "v_add_u32 v220, 25427968, v220\n"
      "s_add_u32 s74, s47, 7\n"
      "v_mfma_f32_16x16x32_bf16 v[12:15], a[64:67], v[144:147], v[12:15]\n"
      "v_mov_b32 v255, s74\n"
      "s_sub_u32 s36, s66, 25165824\n"
      "v_mfma_f32_16x16x32_bf16 v[12:15], a[68:71], v[168:171], v[12:15]\n"
      "s_lshr_b32 s36, s36, 1\n"
      "s_add_u32 s36, s36, 25427968\n"
      "v_mfma_f32_16x16x32_bf16 v[16:19], a[68:71], v[172:175], v[16:19]\n"
      "s_mov_b32 s37, s47\n"
      "s_add_u32 s38, s64, 5\n"
      "v_mfma_f32_16x16x32_bf16 v[16:19], a[64:67], v[148:151], v[16:19]\n"
      "s_sub_u32 s74, s46, 1\n"
      "v_mfma_f32_16x16x32_bf16 v[20:23], a[64:67], v[152:155], v[20:23]\n"
      "s_cmp_eq_u32 s46, 0\n"
      "s_cselect_b32 s74, 6, s74\n"
      "v_mfma_f32_16x16x32_bf16 v[20:23], a[68:71], v[176:179], v[20:23]\n"
      "s_add_u32 s39, s65, s74\n"
      "v_mfma_f32_16x16x32_bf16 v[36:39], a[72:75], v[144:147], v[36:39]\n"
      "v_mfma_f32_16x16x32_bf16 v[36:39], a[76:79], v[168:171], v[36:39]\n"
      "v_mfma_f32_16x16x32_bf16 v[40:43], a[76:79], v[172:175], v[40:43]\n"
      "global_load_lds_dwordx4 v228, s[16:17] offset:1024 nt\n"
      "v_mfma_f32_16x16x32_bf16 v[40:43], a[72:75], v[148:151], v[40:43]\n"
      "v_mfma_f32_16x16x32_bf16 v[44:47], a[72:75], v[152:155], v[44:47]\n"
      "v_mfma_f32_16x16x32_bf16 v[44:47], a[76:79], v[176:179], v[44:47]\n"
      "v_mfma_f32_16x16x32_bf16 v[60:63], a[80:83], v[144:147], v[60:63]\n"
      "v_mfma_f32_16x16x32_bf16 v[60:63], a[84:87], v[168:171], v[60:63]\n"
      "v_mfma_f32_16x16x32_bf16 v[64:67], a[84:87], v[172:175], v[64:67]\n"
      "v_mfma_f32_16x16x32_bf16 v[64:67], a[80:83], v[148:151], v[64:67]\n"
      "v_mfma_f32_16x16x32_bf16 v[68:71], a[80:83], v[152:155], v[68:71]\n"
      "v_mfma_f32_16x16x32_bf16 v[68:71], a[84:87], v[176:179], v[68:71]\n"
      "v_mfma_f32_16x16x32_bf16 v[84:87], a[88:91], v[144:147], v[84:87]\n"
      "global_load_lds_dwordx4 v229, s[16:17] offset:2048 nt\n"
      "v_mfma_f32_16x16x32_bf16 v[84:87], a[92:95], v[168:171], v[84:87]\n"
      "v_mfma_f32_16x16x32_bf16 v[88:91], a[92:95], v[172:175], v[88:91]\n"
      "v_mfma_f32_16x16x32_bf16 v[88:91], a[88:91], v[148:151], v[88:91]\n"
      "v_mfma_f32_16x16x32_bf16 v[92:95], a[88:91], v[152:155], v[92:95]\n"
      "v_mfma_f32_16x16x32_bf16 v[92:95], a[92:95], v[176:179], v[92:95]\n"
      "v_max3_f32 v108, v192, v193, v194\n"
      "v_max3_f32 v112, v196, v197, v198\n"
      "v_max3_f32 v116, v200, v201, v202\n"
      "v_max3_f32 v108, v108, v195, v204\n"
      "v_max3_f32 v112, v112, v199, v208\n"
      "v_max3_f32 v116, v116, v203, v212\n"
      "v_max3_f32 v108, v108, v205, v206\n"
      "v_max3_f32 v112, v112, v209, v210\n"
      "v_max3_f32 v116, v116, v213, v214\n"
      "v_max3_f32 v108, v108, v207, v12\n"
      "v_max3_f32 v112, v112, v211, v16\n"
      "v_max3_f32 v116, v116, v215, v20\n"
      "v_max3_f32 v108, v108, v13, v14\n"
      "v_max3_f32 v112, v112, v17, v18\n"
      "v_max3_f32 v116, v116, v21, v22\n"
      "v_max3_f32 v108, v108, v15, v36\n"
      "v_max3_f32 v112, v112, v19, v40\n"
      "v_max3_f32 v116, v116, v23, v44\n"
      "v_max3_f32 v108, v108, v37, v38\n"
      "v_max3_f32 v112, v112, v41, v42\n"
      "v_max3_f32 v116, v116, v45, v46\n"
      "v_max3_f32 v108, v108, v39, v60\n"
      "v_max3_f32 v112, v112, v43, v64\n"
      "v_max3_f32 v116, v116, v47, v68\n"
      "v_max3_f32 v108, v108, v61, v62\n"
      "v_max3_f32 v112, v112, v65, v66\n"
      "v_max3_f32 v116, v116, v69, v70\n"
      "v_max3_f32 v108, v108, v63, v84\n"
      "v_max3_f32 v112, v112, v67, v88\n"
      "v_max3_f32 v116, v116, v71, v92\n"
      "v_max3_f32 v108, v108, v85, v86\n"
      "v_max3_f32 v112, v112, v89, v90\n"
      "v_max3_f32 v116, v116, v93, v94\n"
      "v_max_f32 v108, v108, v87\n"
      "v_max_f32 v112, v112, v91\n"
      "v_max_f32 v116, v116, v95\n"
      "v_mov_b32 v109, v108\n"
      "v_mov_b32 v113, v112\n"
      "v_mov_b32 v117, v116\n"
      "v_permlane16_swap_b32 v108, v109\n"
      "v_permlane16_swap_b32 v112, v113\n"
      "v_permlane16_swap_b32 v116, v117\n"
      "v_max_f32 v108, v108, v109\n"
      "v_max_f32 v112, v112, v113\n"
      "v_max_f32 v116, v116, v117\n"
      "v_mov_b32 v109, v108\n"
      "v_mov_b32 v113, v112\n"
      "v_mov_b32 v117, v116\n"
      "v_permlane32_swap_b32 v108, v109\n"
      "v_permlane32_swap_b32 v112, v113\n"
      "v_permlane32_swap_b32 v116, v117\n"
      "v_max_f32 v108, v108, v109\n"
      "v_max_f32 v112, v112, v113\n"
      "v_max_f32 v116, v116, v117\n"
      "v_mul_f32 v108, s70, v108\n"
      "v_mul_f32 v112, s70, v112\n"
      "v_mul_f32 v116, s70, v116\n"
      "v_pk_fma_f32 v[192:193], v[192:193], s[70:71], v[108:109] op_sel_hi:[1,1,0] neg_lo:[0,0,1] neg_hi:[0,0,1]\n"
      "v_pk_fma_f32 v[194:195], v[194:195], s[70:71], v[108:109] op_sel_hi:[1,1,0] neg_lo:[0,0,1] neg_hi:[0,0,1]\n"
      "v_pk_fma_f32 v[196:197], v[196:197], s[70:71], v[112:113] op_sel_hi:[1,1,0] neg_lo:[0,0,1] neg_hi:[0,0,1]\n"
      "v_pk_fma_f32 v[198:199], v[198:199], s[70:71], v[112:113] op_sel_hi:[1,1,0] neg_lo:[0,0,1] neg_hi:[0,0,1]\n"
      "v_pk_fma_f32 v[200:201], v[200:201], s[70:71], v[116:117] op_sel_hi:[1,1,0] neg_lo:[0,0,1] neg_hi:[0,0,1]\n"
      "v_pk_fma_f32 v[202:203], v[202:203], s[70:71], v[116:117] op_sel_hi:[1,1,0] neg_lo:[0,0,1] neg_hi:[0,0,1]\n"
      "v_exp_f32 v192, v192\n"
      "v_exp_f32 v193, v193\n"
      "v_exp_f32 v194, v194\n"
      "v_exp_f32 v195, v195\n"
      "v_exp_f32 v196, v196\n"
      "v_exp_f32 v197, v197\n"
      "v_exp_f32 v198, v198\n"
      "v_exp_f32 v199, v199\n"
      "v_exp_f32 v200, v200\n"
      "v_exp_f32 v201, v201\n"
      "v_exp_f32 v202, v202\n"
      "v_exp_f32 v203, v203\n"
      "global_load_lds_dwordx4 v230, s[16:17] offset:3072 nt\n"
      "s_add_u32 s16, s16, 16384\n"
      "s_addc_u32 s17, s17, 0\n"
      "v_pk_add_f32 v[110:111], v[192:193], v[194:195]\n"
      "v_pk_add_f32 v[114:115], v[196:197], v[198:199]\n"
      "v_pk_add_f32 v[118:119], v[200:201], v[202:203]\n"
      "v_pk_fma_f32 v[204:205], v[204:205], s[70:71], v[108:109] op_sel_hi:[1,1,0] neg_lo:[0,0,1] neg_hi:[0,0,1]\n"
      "v_pk_fma_f32 v[206:207], v[206:207], s[70:71], v[108:109] op_sel_hi:[1,1,0] neg_lo:[0,0,1] neg_hi:[0,0,1]\n"
      "v_pk_fma_f32 v[208:209], v[208:209], s[70:71], v[112:113] op_sel_hi:[1,1,0] neg_lo:[0,0,1] neg_hi:[0,0,1]\n"
      "v_pk_fma_f32 v[210:211], v[210:211], s[70:71], v[112:113] op_sel_hi:[1,1,0] neg_lo:[0,0,1] neg_hi:[0,0,1]\n"
      "v_pk_fma_f32 v[212:213], v[212:213], s[70:71], v[116:117] op_sel_hi:[1,1,0] neg_lo:[0,0,1] neg_hi:[0,0,1]\n"
      "v_pk_fma_f32 v[214:215], v[214:215], s[70:71], v[116:117] op_sel_hi:[1,1,0] neg_lo:[0,0,1] neg_hi:[0,0,1]\n"
      "v_exp_f32 v204, v204\n"
      "v_exp_f32 v205, v205\n"
      "v_exp_f32 v206, v206\n"
      "v_exp_f32 v207, v207\n"
      "v_exp_f32 v208, v208\n"
      "v_exp_f32 v209, v209\n"
      "v_exp_f32 v210, v210\n"
      "v_exp_f32 v211, v211\n"
      "v_exp_f32 v212, v212\n"
      "v_exp_f32 v213, v213\n"
      "v_exp_f32 v214, v214\n"
      "v_exp_f32 v215, v215\n"
      "s_add_u32 m0, s30, 94208\n"
      "s_nop 0\n"
      "global_load_lds_dwordx4 v227, s[16:17] offset:0 nt\n"
      "v_pk_add_f32 v[110:111], v[110:111], v[204:205]\n"
      "v_pk_add_f32 v[114:115], v[114:115], v[208:209]\n"
      "v_pk_add_f32 v[118:119], v[118:119], v[212:213]\n"
      "v_pk_add_f32 v[110:111], v[110:111], v[206:207]\n"
      "v_pk_add_f32 v[114:115], v[114:115], v[210:211]\n"
      "v_pk_add_f32 v[118:119], v[118:119], v[214:215]\n"
      "v_pk_fma_f32 v[12:13], v[12:13], s[70:71], v[108:109] op_sel_hi:[1,1,0] neg_lo:[0,0,1] neg_hi:[0,0,1]\n"
      "v_pk_fma_f32 v[14:15], v[14:15], s[70:71], v[108:109] op_sel_hi:[1,1,0] neg_lo:[0,0,1] neg_hi:[0,0,1]\n"
      "v_pk_fma_f32 v[16:17], v[16:17], s[70:71], v[112:113] op_sel_hi:[1,1,0] neg_lo:[0,0,1] neg_hi:[0,0,1]\n"
      "v_pk_fma_f32 v[18:19], v[18:19], s[70:71], v[112:113] op_sel_hi:[1,1,0] neg_lo:[0,0,1] neg_hi:[0,0,1]\n"
      "v_pk_fma_f32 v[20:21], v[20:21], s[70:71], v[116:117] op_sel_hi:[1,1,0] neg_lo:[0,0,1] neg_hi:[0,0,1]\n"
      "v_pk_fma_f32 v[22:23], v[22:23], s[70:71], v[116:117] op_sel_hi:[1,1,0] neg_lo:[0,0,1] neg_hi:[0,0,1]\n"
      "v_exp_f32 v12, v12\n"
      "v_exp_f32 v13, v13\n"
      "v_exp_f32 v14, v14\n"
      "v_exp_f32 v15, v15\n"
      "v_exp_f32 v16, v16\n"
      "v_exp_f32 v17, v17\n"
      "v_exp_f32 v18, v18\n"
      "v_exp_f32 v19, v19\n"
      "v_exp_f32 v20, v20\n"
      "v_exp_f32 v21, v21\n"
      "v_exp_f32 v22, v22\n"
      "v_exp_f32 v23, v23\n"
      "global_load_lds_dwordx4 v228, s[16:17] offset:1024 nt\n"
      "v_pk_add_f32 v[110:111], v[110:111], v[12:13]\n"
      "v_pk_add_f32 v[114:115], v[114:115], v[16:17]\n"
      "v_pk_add_f32 v[118:119], v[118:119], v[20:21]\n"
      "v_pk_add_f32 v[110:111], v[110:111], v[14:15]\n"
      "v_pk_add_f32 v[114:115], v[114:115], v[18:19]\n"
      "v_pk_add_f32 v[118:119], v[118:119], v[22:23]\n"
      "v_pk_fma_f32 v[36:37], v[36:37], s[70:71], v[108:109] op_sel_hi:[1,1,0] neg_lo:[0,0,1] neg_hi:[0,0,1]\n"
      "v_pk_fma_f32 v[38:39], v[38:39], s[70:71], v[108:109] op_sel_hi:[1,1,0] neg_lo:[0,0,1] neg_hi:[0,0,1]\n"
      "v_pk_fma_f32 v[40:41], v[40:41], s[70:71], v[112:113] op_sel_hi:[1,1,0] neg_lo:[0,0,1] neg_hi:[0,0,1]\n"
      "v_pk_fma_f32 v[42:43], v[42:43], s[70:71], v[112:113] op_sel_hi:[1,1,0] neg_lo:[0,0,1] neg_hi:[0,0,1]\n"
      "v_pk_fma_f32 v[44:45], v[44:45], s[70:71], v[116:117] op_sel_hi:[1,1,0] neg_lo:[0,0,1] neg_hi:[0,0,1]\n"
      "v_pk_fma_f32 v[46:47], v[46:47], s[70:71], v[116:117] op_sel_hi:[1,1,0] neg_lo:[0,0,1] neg_hi:[0,0,1]\n"
      "v_exp_f32 v36, v36\n"
      "v_exp_f32 v37, v37\n"
      "v_exp_f32 v38, v38\n"
      "v_exp_f32 v39, v39\n"
      "v_exp_f32 v40, v40\n"
      "v_exp_f32 v41, v41\n"
      "v_exp_f32 v42, v42\n"
      "v_exp_f32 v43, v43\n"
      "v_exp_f32 v44, v44\n"
      "v_exp_f32 v45, v45\n"
      "v_exp_f32 v46, v46\n"
      "v_exp_f32 v47, v47\n"
      "v_pk_add_f32 v[110:111], v[110:111], v[36:37]\n"
      "v_pk_add_f32 v[114:115], v[114:115], v[40:41]\n"
      "v_pk_add_f32 v[118:119], v[118:119], v[44:45]\n"
      "v_pk_add_f32 v[110:111], v[110:111], v[38:39]\n"
      "v_pk_add_f32 v[114:115], v[114:115], v[42:43]\n"
      "v_pk_add_f32 v[118:119], v[118:119], v[46:47]\n"
      "v_pk_fma_f32 v[60:61], v[60:61], s[70:71], v[108:109] op_sel_hi:[1,1,0] neg_lo:[0,0,1] neg_hi:[0,0,1]\n"
      "v_pk_fma_f32 v[62:63], v[62:63], s[70:71], v[108:109] op_sel_hi:[1,1,0] neg_lo:[0,0,1] neg_hi:[0,0,1]\n"
      "v_pk_fma_f32 v[64:65], v[64:65], s[70:71], v[112:113] op_sel_hi:[1,1,0] neg_lo:[0,0,1] neg_hi:[0,0,1]\n"
      "v_pk_fma_f32 v[66:67], v[66:67], s[70:71], v[112:113] op_sel_hi:[1,1,0] neg_lo:[0,0,1] neg_hi:[0,0,1]\n"
      "v_pk_fma_f32 v[68:69], v[68:69], s[70:71], v[116:117] op_sel_hi:[1,1,0] neg_lo:[0,0,1] neg_hi:[0,0,1]\n"
      "v_pk_fma_f32 v[70:71], v[70:71], s[70:71], v[116:117] op_sel_hi:[1,1,0] neg_lo:[0,0,1] neg_hi:[0,0,1]\n"
      "v_exp_f32 v60, v60\n"
      "v_exp_f32 v61, v61\n"
      "v_exp_f32 v62, v62\n"
      "v_exp_f32 v63, v63\n"
      "v_exp_f32 v64, v64\n"
      "v_exp_f32 v65, v65\n"
      "v_exp_f32 v66, v66\n"
      "v_exp_f32 v67, v67\n"
      "v_exp_f32 v68, v68\n"
      "v_exp_f32 v69, v69\n"
      "v_exp_f32 v70, v70\n"
      "v_exp_f32 v71, v71\n"
      "v_pk_add_f32 v[110:111], v[110:111], v[60:61]\n"
      "v_pk_add_f32 v[114:115], v[114:115], v[64:65]\n"
      "v_pk_add_f32 v[118:119], v[118:119], v[68:69]\n"
      "v_pk_add_f32 v[110:111], v[110:111], v[62:63]\n"
      "v_pk_add_f32 v[114:115], v[114:115], v[66:67]\n"
      "v_pk_add_f32 v[118:119], v[118:119], v[70:71]\n"
      "v_pk_fma_f32 v[84:85], v[84:85], s[70:71], v[108:109] op_sel_hi:[1,1,0] neg_lo:[0,0,1] neg_hi:[0,0,1]\n"
      "v_pk_fma_f32 v[86:87], v[86:87], s[70:71], v[108:109] op_sel_hi:[1,1,0] neg_lo:[0,0,1] neg_hi:[0,0,1]\n"
      "v_pk_fma_f32 v[88:89], v[88:89], s[70:71], v[112:113] op_sel_hi:[1,1,0] neg_lo:[0,0,1] neg_hi:[0,0,1]\n"
      "v_pk_fma_f32 v[90:91], v[90:91], s[70:71], v[112:113] op_sel_hi:[1,1,0] neg_lo:[0,0,1] neg_hi:[0,0,1]\n"
      "v_pk_fma_f32 v[92:93], v[92:93], s[70:71], v[116:117] op_sel_hi:[1,1,0] neg_lo:[0,0,1] neg_hi:[0,0,1]\n"
      "v_pk_fma_f32 v[94:95], v[94:95], s[70:71], v[116:117] op_sel_hi:[1,1,0] neg_lo:[0,0,1] neg_hi:[0,0,1]\n"
      "v_exp_f32 v84, v84\n"
      "v_exp_f32 v85, v85\n"
      "v_exp_f32 v86, v86\n"
      "v_exp_f32 v87, v87\n"
      "v_exp_f32 v88, v88\n"
      "v_exp_f32 v89, v89\n"
      "v_exp_f32 v90, v90\n"
      "v_exp_f32 v91, v91\n"
      "v_exp_f32 v92, v92\n"
      "v_exp_f32 v93, v93\n"
      "v_exp_f32 v94, v94\n"
      "v_exp_f32 v95, v95\n"
      "v_pk_add_f32 v[110:111], v[110:111], v[84:85]\n"
      "v_pk_add_f32 v[114:115], v[114:115], v[88:89]\n"
      "v_pk_add_f32 v[118:119], v[118:119], v[92:93]\n"
      "v_pk_add_f32 v[110:111], v[110:111], v[86:87]\n"
      "v_pk_add_f32 v[114:115], v[114:115], v[90:91]\n"
      "v_pk_add_f32 v[118:119], v[118:119], v[94:95]\n"
      "s_waitcnt vmcnt(18)\n"
      "s_barrier\n"
      "v_add_f32 v108, v110, v111\n"
      "v_add_f32 v112, v114, v115\n"
      "v_add_f32 v116, v118, v119\n"
      "ds_read_b64_tr_b16 a[0:1], v245 offset:0\n"
      "v_mov_b32 v109, v108\n"
      "v_mov_b32 v113, v112\n"
      "v_mov_b32 v117, v116\n"
      "ds_read_b64_tr_b16 a[2:3], v245 offset:2048\n"
      "v_permlane16_swap_b32 v108, v109\n"
      "v_permlane16_swap_b32 v112, v113\n"
      "v_permlane16_swap_b32 v116, v117\n"
      "ds_read_b64_tr_b16 a[4:5], v245 offset:4096\n"
      "v_add_f32 v108, v108, v109\n"
      "v_add_f32 v112, v112, v113\n"
      "v_add_f32 v116, v116, v117\n"
      "ds_read_b64_tr_b16 a[6:7], v245 offset:6144\n"
      "v_mov_b32 v109, v108\n"
      "v_mov_b32 v113, v112\n"
      "v_mov_b32 v117, v116\n"
      "ds_read_b64_tr_b16 a[8:9], v245 offset:8192\n"
      "v_permlane32_swap_b32 v108, v109\n"
      "v_permlane32_swap_b32 v112, v113\n"
      "v_permlane32_swap_b32 v116, v117\n"
      "ds_read_b64_tr_b16 a[10:11], v245 offset:10240\n"
      "v_add_f32 v108, v108, v109\n"
      "v_add_f32 v112, v112, v113\n"
      "v_add_f32 v116, v116, v117\n"
      "ds_read_b64_tr_b16 a[12:13], v246 offset:0\n"
      "v_rcp_f32 v108, v108\n"
      "v_rcp_f32 v112, v112\n"
      "v_rcp_f32 v116, v116\n"
      "ds_read_b64_tr_b16 a[14:15], v246 offset:2048\n"
      "v_pk_mul_f32 v[192:193], v[192:193], v[108:109] op_sel_hi:[1,0]\n"
      "v_pk_mul_f32 v[194:195], v[194:195], v[108:109] op_sel_hi:[1,0]\n"
      "v_pk_mul_f32 v[204:205], v[204:205], v[108:109] op_sel_hi:[1,0]\n"
      "ds_read_b64_tr_b16 a[16:17], v246 offset:4096\n"
      "v_pk_mul_f32 v[206:207], v[206:207], v[108:109] op_sel_hi:[1,0]\n"
      "v_cvt_pk_bf16_f32 v192, v192, v193\n"
      "v_cvt_pk_bf16_f32 v193, v194, v195\n"
      "ds_read_b64_tr_b16 a[18:19], v246 offset:6144\n"
      "v_cvt_pk_bf16_f32 v194, v204, v205\n"
      "v_cvt_pk_bf16_f32 v195, v206, v207\n"
      "v_pk_mul_f32 v[196:197], v[196:197], v[112:113] op_sel_hi:[1,0]\n"
      "ds_read_b64_tr_b16 a[20:21], v246 offset:8192\n"
      "v_pk_mul_f32 v[198:199], v[198:199], v[112:113] op_sel_hi:[1,0]\n"
      "v_pk_mul_f32 v[208:209], v[208:209], v[112:113] op_sel_hi:[1,0]\n"
      "v_pk_mul_f32 v[210:211], v[210:211], v[112:113] op_sel_hi:[1,0]\n"
      "ds_read_b64_tr_b16 a[22:23], v246 offset:10240\n"
      "v_cvt_pk_bf16_f32 v196, v196, v197\n"
      "v_cvt_pk_bf16_f32 v197, v198, v199\n"
      "v_cvt_pk_bf16_f32 v198, v208, v209\n"
      "ds_read_b64_tr_b16 a[24:25], v247 offset:0\n"
      "v_cvt_pk_bf16_f32 v199, v210, v211\n"
      "v_pk_mul_f32 v[200:201], v[200:201], v[116:117] op_sel_hi:[1,0]\n"
      "v_pk_mul_f32 v[202:203], v[202:203], v[116:117] op_sel_hi:[1,0]\n"
      "ds_read_b64_tr_b16 a[26:27], v247 offset:2048\n"
      "v_pk_mul_f32 v[212:213], v[212:213], v[116:117] op_sel_hi:[1,0]\n"
      "v_pk_mul_f32 v[214:215], v[214:215], v[116:117] op_sel_hi:[1,0]\n"
      "v_cvt_pk_bf16_f32 v200, v200, v201\n"
      "ds_read_b64_tr_b16 a[28:29], v247 offset:4096\n"
      "v_cvt_pk_bf16_f32 v201, v202, v203\n"
      "v_cvt_pk_bf16_f32 v202, v212, v213\n"
      "v_cvt_pk_bf16_f32 v203, v214, v215\n"
      "ds_read_b64_tr_b16 a[30:31], v247 offset:6144\n"
      "v_pk_mul_f32 v[12:13], v[12:13], v[108:109] op_sel_hi:[1,0]\n"
      "v_pk_mul_f32 v[14:15], v[14:15], v[108:109] op_sel_hi:[1,0]\n"
      "v_pk_mul_f32 v[36:37], v[36:37], v[108:109] op_sel_hi:[1,0]\n"
      "ds_read_b64_tr_b16 a[32:33], v247 offset:8192\n"
      "v_pk_mul_f32 v[38:39], v[38:39], v[108:109] op_sel_hi:[1,0]\n"
      "v_cvt_pk_bf16_f32 v12, v12, v13\n"
      "v_cvt_pk_bf16_f32 v13, v14, v15\n"
      "ds_read_b64_tr_b16 a[34:35], v247 offset:10240\n"
      "v_cvt_pk_bf16_f32 v14, v36, v37\n"
      "v_cvt_pk_bf16_f32 v15, v38, v39\n"
      "v_pk_mul_f32 v[16:17], v[16:17], v[112:113] op_sel_hi:[1,0]\n"
      "ds_read_b64_tr_b16 a[36:37], v248 offset:0\n"
      "v_pk_mul_f32 v[18:19], v[18:19], v[112:113] op_sel_hi:[1,0]\n"
      "v_pk_mul_f32 v[40:41], v[40:41], v[112:113] op_sel_hi:[1,0]\n"
      "v_pk_mul_f32 v[42:43], v[42:43], v[112:113] op_sel_hi:[1,0]\n"
      "ds_read_b64_tr_b16 a[38:39], v248 offset:2048\n"
      "v_cvt_pk_bf16_f32 v16, v16, v17\n"
      "v_cvt_pk_bf16_f32 v17, v18, v19\n"
      "v_cvt_pk_bf16_f32 v18, v40, v41\n"
      "ds_read_b64_tr_b16 a[40:41], v248 offset:4096\n"
      "v_cvt_pk_bf16_f32 v19, v42, v43\n"
      "v_pk_mul_f32 v[20:21], v[20:21], v[116:117] op_sel_hi:[1,0]\n"
      "v_pk_mul_f32 v[22:23], v[22:23], v[116:117] op_sel_hi:[1,0]\n"
      "ds_read_b64_tr_b16 a[42:43], v248 offset:6144\n"
      "v_pk_mul_f32 v[44:45], v[44:45], v[116:117] op_sel_hi:[1,0]\n"
      "v_pk_mul_f32 v[46:47], v[46:47], v[116:117] op_sel_hi:[1,0]\n"
      "v_cvt_pk_bf16_f32 v20, v20, v21\n"
      "ds_read_b64_tr_b16 a[44:45], v248 offset:8192\n"
      "v_cvt_pk_bf16_f32 v21, v22, v23\n"
      "v_cvt_pk_bf16_f32 v22, v44, v45\n"
      "v_cvt_pk_bf16_f32 v23, v46, v47\n"
      "ds_read_b64_tr_b16 a[46:47], v248 offset:10240\n"
      "v_pk_mul_f32 v[60:61], v[60:61], v[108:109] op_sel_hi:[1,0]\n"
      "v_pk_mul_f32 v[62:63], v[62:63], v[108:109] op_sel_hi:[1,0]\n"
      "v_pk_mul_f32 v[84:85], v[84:85], v[108:109] op_sel_hi:[1,0]\n"
      "v_pk_mul_f32 v[86:87], v[86:87], v[108:109] op_sel_hi:[1,0]\n"
      "v_cvt_pk_bf16_f32 v60, v60, v61\n"
      "v_cvt_pk_bf16_f32 v61, v62, v63\n"
      "v_cvt_pk_bf16_f32 v62, v84, v85\n"
      "v_cvt_pk_bf16_f32 v63, v86, v87\n"
      "v_pk_mul_f32 v[64:65], v[64:65], v[112:113] op_sel_hi:[1,0]\n"
      "v_pk_mul_f32 v[66:67], v[66:67], v[112:113] op_sel_hi:[1,0]\n"
      "v_pk_mul_f32 v[88:89], v[88:89], v[112:113] op_sel_hi:[1,0]\n"
      "v_pk_mul_f32 v[90:91], v[90:91], v[112:113] op_sel_hi:[1,0]\n"
      "v_cvt_pk_bf16_f32 v64, v64, v65\n"
      "v_cvt_pk_bf16_f32 v65, v66, v67\n"
      "v_cvt_pk_bf16_f32 v66, v88, v89\n"
      "v_cvt_pk_bf16_f32 v67, v90, v91\n"
      "v_pk_mul_f32 v[68:69], v[68:69], v[116:117] op_sel_hi:[1,0]\n"
      "v_pk_mul_f32 v[70:71], v[70:71], v[116:117] op_sel_hi:[1,0]\n"
      "v_pk_mul_f32 v[92:93], v[92:93], v[116:117] op_sel_hi:[1,0]\n"
      "v_pk_mul_f32 v[94:95], v[94:95], v[116:117] op_sel_hi:[1,0]\n"
      "v_cvt_pk_bf16_f32 v68, v68, v69\n"
      "v_cvt_pk_bf16_f32 v69, v70, v71\n"
      "v_cvt_pk_bf16_f32 v70, v92, v93\n"
      "v_cvt_pk_bf16_f32 v71, v94, v95\n"
      "s_waitcnt vmcnt(12) lgkmcnt(0)\n"
      "s_barrier\n"
      "v_mfma_f32_16x16x32_bf16 v[0:3], a[0:3], v[192:195], 0\n"
      "ds_read_b64_tr_b16 a[48:49], v245 offset:24576\n"
      "v_mfma_f32_16x16x32_bf16 v[0:3], a[4:7], v[12:15], v[0:3]\n"
      "ds_read_b64_tr_b16 a[50:51], v245 offset:26624\n"
      "v_mfma_f32_16x16x32_bf16 v[0:3], a[8:11], v[60:63], v[0:3]\n"
      "ds_read_b64_tr_b16 a[52:53], v245 offset:28672\n"
      "v_mfma_f32_16x16x32_bf16 v[4:7], a[8:11], v[64:67], 0\n"
      "ds_read_b64_tr_b16 a[54:55], v245 offset:30720\n"
      "v_mfma_f32_16x16x32_bf16 v[4:7], a[4:7], v[16:19], v[4:7]\n"
      "ds_read_b64_tr_b16 a[56:57], v245 offset:32768\n"
      "ds_read_b64_tr_b16 a[58:59], v245 offset:34816\n"
      "v_mfma_f32_16x16x32_bf16 v[4:7], a[0:3], v[196:199], v[4:7]\n"
      "ds_read_b64_tr_b16 a[60:61], v246 offset:24576\n"
      "v_mfma_f32_16x16x32_bf16 v[8:11], a[0:3], v[200:203], 0\n"
      "ds_read_b64_tr_b16 a[62:63], v246 offset:26624\n"
      "v_mfma_f32_16x16x32_bf16 v[8:11], a[4:7], v[20:23], v[8:11]\n"
      "ds_read_b64_tr_b16 a[64:65], v246 offset:28672\n"
      "v_mfma_f32_16x16x32_bf16 v[8:11], a[8:11], v[68:71], v[8:11]\n"
      "v_mfma_f32_16x16x32_bf16 v[108:111], a[12:15], v[192:195], 0\n"
      "ds_read_b64_tr_b16 a[66:67], v246 offset:30720\n"
      "v_mfma_f32_16x16x32_bf16 v[108:111], a[16:19], v[12:15], v[108:111]\n"
      "ds_read_b64_tr_b16 a[68:69], v246 offset:32768\n"
      "ds_read_b64_tr_b16 a[70:71], v246 offset:34816\n"
      "v_mfma_f32_16x16x32_bf16 v[108:111], a[20:23], v[60:63], v[108:111]\n"
      "ds_read_b64_tr_b16 a[72:73], v247 offset:24576\n"
      "v_mfma_f32_16x16x32_bf16 v[112:115], a[20:23], v[64:67], 0\n"
      "ds_read_b64_tr_b16 a[74:75], v247 offset:26624\n"
      "v_mfma_f32_16x16x32_bf16 v[112:115], a[16:19], v[16:19], v[112:115]\n"
      "ds_read_b64_tr_b16 a[76:77], v247 offset:28672\n"
      "v_mfma_f32_16x16x32_bf16 v[112:115], a[12:15], v[196:199], v[112:115]\n"
      "ds_read_b64_tr_b16 a[78:79], v247 offset:30720\n"
      "v_mfma_f32_16x16x32_bf16 v[116:119], a[12:15], v[200:203], 0\n"
      "ds_read_b64_tr_b16 a[80:81], v247 offset:32768\n"
      "v_mfma_f32_16x16x32_bf16 v[116:119], a[16:19], v[20:23], v[116:119]\n"
      "ds_read_b64_tr_b16 a[82:83], v247 offset:34816\n"
      "v_mfma_f32_16x16x32_bf16 v[116:119], a[20:23], v[68:71], v[116:119]\n"
      "ds_read_b64_tr_b16 a[84:85], v248 offset:24576\n"
      "v_mfma_f32_16x16x32_bf16 v[24:27], a[24:27], v[192:195], 0\n"
      "ds_read_b64_tr_b16 a[86:87], v248 offset:26624\n"
      "v_mfma_f32_16x16x32_bf16 v[24:27], a[28:31], v[12:15], v[24:27]\n"
      "ds_read_b64_tr_b16 a[88:89], v248 offset:28672\n"
      "v_mfma_f32_16x16x32_bf16 v[24:27], a[32:35], v[60:63], v[24:27]\n"
      "v_cvt_pk_bf16_f32 v0, v0, v1\n"
      "v_cvt_pk_bf16_f32 v1, v2, v3\n"
      "v_mfma_f32_16x16x32_bf16 v[28:31], a[32:35], v[64:67], 0\n"
      "v_cvt_pk_bf16_f32 v2, v108, v109\n"
      "ds_read_b64_tr_b16 a[90:91], v248 offset:30720\n"
      "v_mfma_f32_16x16x32_bf16 v[28:31], a[28:31], v[16:19], v[28:31]\n"
      "ds_read_b64_tr_b16 a[92:93], v248 offset:32768\n"
      "v_mfma_f32_16x16x32_bf16 v[28:31], a[24:27], v[196:199], v[28:31]\n"
      "v_mfma_f32_16x16x32_bf16 v[32:35], a[24:27], v[200:203], 0\n"
      "v_cvt_pk_bf16_f32 v3, v110, v111\n"
      "ds_read_b64_tr_b16 a[94:95], v248 offset:34816\n"
      "v_mfma_f32_16x16x32_bf16 v[32:35], a[28:31], v[20:23], v[32:35]\n"
      "ds_write_b128 v251, v[0:3] offset:0\n"
      "v_mfma_f32_16x16x32_bf16 v[32:35], a[32:35], v[68:71], v[32:35]\n"
      "v_cvt_pk_bf16_f32 v4, v4, v5\n"
      "v_cvt_pk_bf16_f32 v5, v6, v7\n"
      "v_mfma_f32_16x16x32_bf16 v[132:135], a[36:39], v[192:195], 0\n"
      "v_cvt_pk_bf16_f32 v6, v112, v113\n"
      "v_cvt_pk_bf16_f32 v7, v114, v115\n"
      "v_mfma_f32_16x16x32_bf16 v[132:135], a[40:43], v[12:15], v[132:135]\n"
      "ds_write_b128 v251, v[4:7] offset:1024\n"
      "v_mfma_f32_16x16x32_bf16 v[132:135], a[44:47], v[60:63], v[132:135]\n"
      "v_cvt_pk_bf16_f32 v8, v8, v9\n"
      "v_cvt_pk_bf16_f32 v9, v10, v11\n"
      "v_mfma_f32_16x16x32_bf16 v[136:139], a[44:47], v[64:67], 0\n"
      "v_mfma_f32_16x16x32_bf16 v[136:139], a[40:43], v[16:19], v[136:139]\n"
      "v_cvt_pk_bf16_f32 v10, v116, v117\n"
      "v_mfma_f32_16x16x32_bf16 v[136:139], a[36:39], v[196:199], v[136:139]\n"
      "v_cvt_pk_bf16_f32 v11, v118, v119\n"
      "v_mfma_f32_16x16x32_bf16 v[140:143], a[36:39], v[200:203], 0\n"
      "ds_write_b128 v251, v[8:11] offset:2048\n"
      "v_mfma_f32_16x16x32_bf16 v[140:143], a[40:43], v[20:23], v[140:143]\n"
      "s_waitcnt vmcnt(6) lgkmcnt(3)\n"
      "v_mfma_f32_16x16x32_bf16 v[140:143], a[44:47], v[68:71], v[140:143]\n"
      "s_barrier\n"
      "v_mfma_f32_16x16x32_bf16 v[48:51], a[48:51], v[192:195], 0\n"
      "v_add_u32 v245, 0x10000, v245\n"
      "v_add_u32 v246, 0x10000, v246\n"
      "v_mfma_f32_16x16x32_bf16 v[48:51], a[52:55], v[12:15], v[48:51]\n"
      "v_add_u32 v247, 0x10000, v247\n"
      "v_add_u32 v248, 0x10000, v248\n"
      "ds_read_b64_tr_b16 a[0:1], v245 offset:0\n"
      "v_mfma_f32_16x16x32_bf16 v[48:51], a[56:59], v[60:63], v[48:51]\n"
      "v_cvt_pk_bf16_f32 v24, v24, v25\n"
      "v_cvt_pk_bf16_f32 v25, v26, v27\n"
      "v_cvt_pk_bf16_f32 v26, v132, v133\n"
      "ds_read_b64_tr_b16 a[2:3], v245 offset:2048\n"
      "v_mfma_f32_16x16x32_bf16 v[52:55], a[56:59], v[64:67], 0\n"
      "s_add_u32 s3, s69, s22\n"
      "s_add_u32 s14, s12, s3\n"
      "s_addc_u32 s15, s13, 0\n"
      "s_add_u32 m0, s28, 49152\n"
      "v_cvt_pk_bf16_f32 v27, v134, v135\n"
      "ds_read_b64_tr_b16 a[4:5], v245 offset:4096\n"
      "v_mfma_f32_16x16x32_bf16 v[52:55], a[52:55], v[16:19], v[52:55]\n"
      "ds_write_b128 v251, v[24:27] offset:3072\n"
      "v_mfma_f32_16x16x32_bf16 v[52:55], a[48:51], v[196:199], v[52:55]\n"
      "v_cvt_pk_bf16_f32 v28, v28, v29\n"
      "v_cvt_pk_bf16_f32 v29, v30, v31\n"
      "ds_read_b64_tr_b16 a[6:7], v245 offset:6144\n"
      "ds_read_b64_tr_b16 a[8:9], v245 offset:8192\n"
      "v_mfma_f32_16x16x32_bf16 v[56:59], a[48:51], v[200:203], 0\n"
      "v_cvt_pk_bf16_f32 v30, v136, v137\n"
      "v_cvt_pk_bf16_f32 v31, v138, v139\n"
      "v_mfma_f32_16x16x32_bf16 v[56:59], a[52:55], v[20:23], v[56:59]\n"
      "ds_write_b128 v251, v[28:31] offset:4096\n"
      "v_mfma_f32_16x16x32_bf16 v[56:59], a[56:59], v[68:71], v[56:59]\n"
      "v_cvt_pk_bf16_f32 v32, v32, v33\n"
      "v_cvt_pk_bf16_f32 v33, v34, v35\n"
      "v_mfma_f32_16x16x32_bf16 v[156:159], a[60:63], v[192:195], 0\n"
      "v_cvt_pk_bf16_f32 v34, v140, v141\n"
      "ds_read_b64_tr_b16 a[10:11], v245 offset:10240\n"
      "v_mfma_f32_16x16x32_bf16 v[156:159], a[64:67], v[12:15], v[156:159]\n"
      "ds_read_b64_tr_b16 a[12:13], v246 offset:0\n"
      "ds_read_b64_tr_b16 a[14:15], v246 offset:2048\n"
      "v_mfma_f32_16x16x32_bf16 v[156:159], a[68:71], v[60:63], v[156:159]\n"
      "ds_read_b64_tr_b16 a[16:17], v246 offset:4096\n"
      "ds_read_b64_tr_b16 a[18:19], v246 offset:6144\n"
      "v_mfma_f32_16x16x32_bf16 v[160:163], a[68:71], v[64:67], 0\n"
      "v_cvt_pk_bf16_f32 v35, v142, v143\n"
      "ds_read_b64_tr_b16 a[20:21], v246 offset:8192\n"
      "ds_read_b64_tr_b16 a[22:23], v246 offset:10240\n"
      "ds_read_b64_tr_b16 a[24:25], v247 offset:0\n"
      "v_mfma_f32_16x16x32_bf16 v[160:163], a[64:67], v[16:19], v[160:163]\n"
      "global_load_lds_dwordx4 v231, s[14:15] offset:0\n"
      "v_mfma_f32_16x16x32_bf16 v[160:163], a[60:63], v[196:199], v[160:163]\n"
      "global_load_lds_dwordx4 v232, s[14:15] offset:1024\n"
      "v_mfma_f32_16x16x32_bf16 v[164:167], a[60:63], v[200:203], 0\n"
      "ds_read_b64_tr_b16 a[26:27], v247 offset:2048\n"
      "ds_read_b64_tr_b16 a[28:29], v247 offset:4096\n"
      "v_mfma_f32_16x16x32_bf16 v[164:167], a[64:67], v[20:23], v[164:167]\n"
      "global_load_lds_dwordx4 v233, s[14:15] offset:2048\n"
      "v_mfma_f32_16x16x32_bf16 v[164:167], a[68:71], v[68:71], v[164:167]\n"
      "ds_write_b128 v251, v[32:35] offset:5120\n"
      "v_mfma_f32_16x16x32_bf16 v[72:75], a[72:75], v[192:195], 0\n"
      "global_load_lds_dwordx4 v234, s[14:15] offset:3072\n"
      "v_mfma_f32_16x16x32_bf16 v[72:75], a[76:79], v[12:15], v[72:75]\n"
      "ds_read_b64_tr_b16 a[30:31], v247 offset:6144\n"
      "ds_read_b64_tr_b16 a[32:33], v247 offset:8192\n"
      "v_mfma_f32_16x16x32_bf16 v[72:75], a[80:83], v[60:63], v[72:75]\n"
      "s_add_u32 s3, s69, s23\n"
      "v_cvt_pk_bf16_f32 v48, v48, v49\n"
      "v_mfma_f32_16x16x32_bf16 v[76:79], a[80:83], v[64:67], 0\n"
      "s_add_u32 s14, s12, s3\n"
      "s_addc_u32 s15, s13, 0\n"
      "ds_read_b64_tr_b16 a[34:35], v247 offset:10240\n"
      "v_mfma_f32_16x16x32_bf16 v[76:79], a[76:79], v[16:19], v[76:79]\n"
      "v_cvt_pk_bf16_f32 v49, v50, v51\n"
      "ds_read_b64_tr_b16 a[36:37], v248 offset:0\n"
      "v_mfma_f32_16x16x32_bf16 v[76:79], a[72:75], v[196:199], v[76:79]\n"
      "v_cvt_pk_bf16_f32 v50, v156, v157\n"
      "ds_read_b64_tr_b16 a[38:39], v248 offset:2048\n"
      "v_mfma_f32_16x16x32_bf16 v[80:83], a[72:75], v[200:203], 0\n"
      "v_cvt_pk_bf16_f32 v51, v158, v159\n"
      "ds_read_b64_tr_b16 a[40:41], v248 offset:4096\n"
      "v_mfma_f32_16x16x32_bf16 v[80:83], a[76:79], v[20:23], v[80:83]\n"
      "ds_write_b128 v251, v[48:51] offset:24576\n"
      "v_mfma_f32_16x16x32_bf16 v[80:83], a[80:83], v[68:71], v[80:83]\n"
      "s_add_u32 m0, s28, 114688\n"
      "v_cvt_pk_bf16_f32 v52, v52, v53\n"
      "v_mfma_f32_16x16x32_bf16 v[180:183], a[84:87], v[192:195], 0\n"
      "ds_read_b64_tr_b16 a[42:43], v248 offset:6144\n"
      "ds_read_b64_tr_b16 a[44:45], v248 offset:8192\n"
      "v_mfma_f32_16x16x32_bf16 v[180:183], a[88:91], v[12:15], v[180:183]\n"
      "global_load_lds_dwordx4 v231, s[14:15] offset:0\n"
      "v_mfma_f32_16x16x32_bf16 v[180:183], a[92:95], v[60:63], v[180:183]\n"
      "v_cvt_pk_bf16_f32 v53, v54, v55\n"
      "ds_read_b64_tr_b16 a[46:47], v248 offset:10240\n"
      "v_mfma_f32_16x16x32_bf16 v[184:187], a[92:95], v[64:67], 0\n"
      "v_cvt_pk_bf16_f32 v54, v160, v161\n"
      "v_cvt_pk_bf16_f32 v55, v162, v163\n"
      "v_mfma_f32_16x16x32_bf16 v[184:187], a[88:91], v[16:19], v[184:187]\n"
      "ds_write_b128 v251, v[52:55] offset:25600\n"
      "v_mfma_f32_16x16x32_bf16 v[184:187], a[84:87], v[196:199], v[184:187]\n"
      "v_cvt_pk_bf16_f32 v56, v56, v57\n"
      "v_cvt_pk_bf16_f32 v57, v58, v59\n"
      "v_mfma_f32_16x16x32_bf16 v[188:191], a[84:87], v[200:203], 0\n"
      "global_load_lds_dwordx4 v232, s[14:15] offset:1024\n"
      "v_mfma_f32_16x16x32_bf16 v[188:191], a[88:91], v[20:23], v[188:191]\n"
      "v_cvt_pk_bf16_f32 v58, v164, v165\n"
      "v_cvt_pk_bf16_f32 v59, v166, v167\n"
      "s_waitcnt vmcnt(6) lgkmcnt(1)\n"
      "v_mfma_f32_16x16x32_bf16 v[188:191], a[92:95], v[68:71], v[188:191]\n"
      "ds_write_b128 v251, v[56:59] offset:26624\n"
      "s_barrier\n"
      "v_mfma_f32_16x16x32_bf16 v[96:99], a[0:3], v[192:195], 0\n"
      "global_load_lds_dwordx4 v233, s[14:15] offset:2048\n"
      "v_mfma_f32_16x16x32_bf16 v[96:99], a[4:7], v[12:15], v[96:99]\n"
      "ds_read_b64_tr_b16 a[48:49], v245 offset:24576\n"
      "ds_read_b64_tr_b16 a[50:51], v245 offset:26624\n"
      "ds_read_b64_tr_b16 a[52:53], v245 offset:28672\n"
      "v_mfma_f32_16x16x32_bf16 v[96:99], a[8:11], v[60:63], v[96:99]\n"
      "ds_read_b64_tr_b16 a[54:55], v245 offset:30720\n"
      "ds_read_b64_tr_b16 a[56:57], v245 offset:32768\n"
      "v_mfma_f32_16x16x32_bf16 v[100:103], a[8:11], v[64:67], 0\n"
      "v_cvt_pk_bf16_f32 v72, v72, v73\n"
      "v_cvt_pk_bf16_f32 v73, v74, v75\n"
      "v_mfma_f32_16x16x32_bf16 v[100:103], a[4:7], v[16:19], v[100:103]\n"
      "v_cvt_pk_bf16_f32 v74, v180, v181\n"
      "v_cvt_pk_bf16_f32 v75, v182, v183\n"
      "v_mfma_f32_16x16x32_bf16 v[100:103], a[0:3], v[196:199], v[100:103]\n"
      "ds_write_b128 v251, v[72:75] offset:27648\n"
      "v_mfma_f32_16x16x32_bf16 v[104:107], a[0:3], v[200:203], 0\n"
      "v_cvt_pk_bf16_f32 v76, v76, v77\n"
      "ds_read_b64_tr_b16 a[58:59], v245 offset:34816\n"
      "v_mfma_f32_16x16x32_bf16 v[104:107], a[4:7], v[20:23], v[104:107]\n"
      "v_cvt_pk_bf16_f32 v77, v78, v79\n"
      "ds_read_b64_tr_b16 a[60:61], v246 offset:24576\n"
      "v_mfma_f32_16x16x32_bf16 v[104:107], a[8:11], v[68:71], v[104:107]\n"
      "global_load_lds_dwordx4 v234, s[14:15] offset:3072\n"
      "v_mfma_f32_16x16x32_bf16 v[108:111], a[12:15], v[192:195], 0\n"
      "v_cvt_pk_bf16_f32 v78, v184, v185\n"
      "ds_read_b64_tr_b16 a[62:63], v246 offset:26624\n"
      "v_mfma_f32_16x16x32_bf16 v[108:111], a[16:19], v[12:15], v[108:111]\n"
      "v_cvt_pk_bf16_f32 v79, v186, v187\n"
      "ds_read_b64_tr_b16 a[64:65], v246 offset:28672\n"
      "v_mfma_f32_16x16x32_bf16 v[108:111], a[20:23], v[60:63], v[108:111]\n"
      "ds_write_b128 v251, v[76:79] offset:28672\n"
      "v_mfma_f32_16x16x32_bf16 v[112:115], a[20:23], v[64:67], 0\n"
      "s_add_u32 s3, s69, s34\n"
      "ds_read_b64_tr_b16 a[66:67], v246 offset:30720\n"
      "v_mfma_f32_16x16x32_bf16 v[112:115], a[16:19], v[16:19], v[112:115]\n"
      "v_cvt_pk_bf16_f32 v80, v80, v81\n"
      "ds_read_b64_tr_b16 a[68:69], v246 offset:32768\n"
      "v_mfma_f32_16x16x32_bf16 v[112:115], a[12:15], v[196:199], v[112:115]\n"
      "s_add_u32 s14, s12, s3\n"
      "s_addc_u32 s15, s13, 0\n"
      "ds_read_b64_tr_b16 a[70:71], v246 offset:34816\n"
      "v_mfma_f32_16x16x32_bf16 v[116:119], a[12:15], v[200:203], 0\n"
      "v_cvt_pk_bf16_f32 v81, v82, v83\n"
      "v_cvt_pk_bf16_f32 v82, v188, v189\n"
      "ds_read_b64_tr_b16 a[72:73], v247 offset:24576\n"
      "v_mfma_f32_16x16x32_bf16 v[116:119], a[16:19], v[20:23], v[116:119]\n"
      "v_cvt_pk_bf16_f32 v83, v190, v191\n"
      "ds_read_b64_tr_b16 a[74:75], v247 offset:26624\n"
      "v_mfma_f32_16x16x32_bf16 v[116:119], a[20:23], v[68:71], v[116:119]\n"
      "ds_write_b128 v251, v[80:83] offset:29696\n"
      "v_mfma_f32_16x16x32_bf16 v[120:123], a[24:27], v[192:195], 0\n"
      "s_add_u32 m0, s28, 131072\n"
      "ds_read_b64_tr_b16 a[76:77], v247 offset:28672\n"
      "v_mfma_f32_16x16x32_bf16 v[120:123], a[28:31], v[12:15], v[120:123]\n"
      "global_load_lds_dwordx4 v231, s[14:15] offset:0\n"
      "v_mfma_f32_16x16x32_bf16 v[120:123], a[32:35], v[60:63], v[120:123]\n"
      "v_add_u32 v251, 0x10000, v251\n"
      "ds_read_b64_tr_b16 a[78:79], v247 offset:30720\n"
      "v_mfma_f32_16x16x32_bf16 v[124:127], a[32:35], v[64:67], 0\n"
      "v_cvt_pk_bf16_f32 v96, v96, v97\n"
      "v_cvt_pk_bf16_f32 v97, v98, v99\n"
      "v_cvt_pk_bf16_f32 v98, v108, v109\n"
      "ds_read_b64_tr_b16 a[80:81], v247 offset:32768\n"
      "v_mfma_f32_16x16x32_bf16 v[124:127], a[28:31], v[16:19], v[124:127]\n"
      "v_cvt_pk_bf16_f32 v99, v110, v111\n"
      "ds_read_b64_tr_b16 a[82:83], v247 offset:34816\n"
      "ds_read_b64_tr_b16 a[84:85], v248 offset:24576\n"
      "v_mfma_f32_16x16x32_bf16 v[124:127], a[24:27], v[196:199], v[124:127]\n"
      "ds_read_b64_tr_b16 a[86:87], v248 offset:26624\n"
      "ds_read_b64_tr_b16 a[88:89], v248 offset:28672\n"
      "v_mfma_f32_16x16x32_bf16 v[128:131], a[24:27], v[200:203], 0\n"
      "global_load_lds_dwordx4 v232, s[14:15] offset:1024\n"
      "v_mfma_f32_16x16x32_bf16 v[128:131], a[28:31], v[20:23], v[128:131]\n"
      "ds_write_b128 v251, v[96:99] offset:0\n"
      "v_mfma_f32_16x16x32_bf16 v[128:131], a[32:35], v[68:71], v[128:131]\n"
      "v_cvt_pk_bf16_f32 v100, v100, v101\n"
      "v_cvt_pk_bf16_f32 v101, v102, v103\n"
      "ds_read_b64_tr_b16 a[90:91], v248 offset:30720\n"
      "ds_read_b64_tr_b16 a[92:93], v248 offset:32768\n"
      "ds_read_b64_tr_b16 a[94:95], v248 offset:34816\n"
      "v_mfma_f32_16x16x32_bf16 v[132:135], a[36:39], v[192:195], 0\n"
      "global_load_lds_dwordx4 v233, s[14:15] offset:2048\n"
      "v_mfma_f32_16x16x32_bf16 v[132:135], a[40:43], v[12:15], v[132:135]\n"
      "v_cvt_pk_bf16_f32 v102, v112, v113\n"
      "ds_read_b128 a[96:99], v252 offset:0\n"
      "ds_read_b128 a[100:103], v252 offset:1024\n"
      "ds_read_b128 a[104:107], v252 offset:2048\n"
      "v_mfma_f32_16x16x32_bf16 v[132:135], a[44:47], v[60:63], v[132:135]\n"
      "v_cvt_pk_bf16_f32 v103, v114, v115\n"
      "ds_read_b128 a[108:111], v252 offset:3072\n"
      "ds_read_b128 a[112:115], v252 offset:4096\n"
      "v_mfma_f32_16x16x32_bf16 v[136:139], a[44:47], v[64:67], 0\n"
      "ds_write_b128 v251, v[100:103] offset:1024\n"
      "v_mfma_f32_16x16x32_bf16 v[136:139], a[40:43], v[16:19], v[136:139]\n"
      "global_load_lds_dwordx4 v234, s[14:15] offset:3072\n"
      "v_mfma_f32_16x16x32_bf16 v[136:139], a[36:39], v[196:199], v[136:139]\n"
      "s_add_u32 s3, s69, s35\n"
      "v_cvt_pk_bf16_f32 v104, v104, v105\n"
      "ds_read_b128 a[116:119], v252 offset:5120\n"
      "v_mfma_f32_16x16x32_bf16 v[140:143], a[36:39], v[200:203], 0\n"
      "s_add_u32 s14, s12, s3\n"
      "s_addc_u32 s15, s13, 0\n"
      "v_mfma_f32_16x16x32_bf16 v[140:143], a[40:43], v[20:23], v[140:143]\n"
      "v_cvt_pk_bf16_f32 v105, v106, v107\n"
      "v_cvt_pk_bf16_f32 v106, v116, v117\n"
      "v_cvt_pk_bf16_f32 v107, v118, v119\n"
      "s_waitcnt vmcnt(8) lgkmcnt(0)\n"
      "v_mfma_f32_16x16x32_bf16 v[140:143], a[44:47], v[68:71], v[140:143]\n"
      "ds_write_b128 v251, v[104:107] offset:2048\n"
      "s_barrier\n"
      "v_mfma_f32_16x16x32_bf16 v[144:147], a[48:51], v[192:195], 0\n"
      "ds_read_b64_tr_b16 a[192:193], v249 offset:49152\n"
      "v_mfma_f32_16x16x32_bf16 v[144:147], a[52:55], v[12:15], v[144:147]\n"
      "s_add_u32 m0, s28, 147456\n"
      "ds_read_b64_tr_b16 a[194:195], v249 offset:51200\n"
      "v_mfma_f32_16x16x32_bf16 v[144:147], a[56:59], v[60:63], v[144:147]\n"
      "v_cvt_pk_bf16_f32 v120, v120, v121\n"
      "ds_read_b64_tr_b16 a[196:197], v249 offset:53248\n"
      "v_mfma_f32_16x16x32_bf16 v[148:151], a[56:59], v[64:67], 0\n"
      "v_cvt_pk_bf16_f32 v121, v122, v123\n"
      "v_cvt_pk_bf16_f32 v122, v132, v133\n"
      "v_mfma_f32_16x16x32_bf16 v[148:151], a[52:55], v[16:19], v[148:151]\n"
      "v_cvt_pk_bf16_f32 v123, v134, v135\n"
      "ds_read_b64_tr_b16 a[198:199], v249 offset:55296\n"
      "v_mfma_f32_16x16x32_bf16 v[148:151], a[48:51], v[196:199], v[148:151]\n"
      "ds_write_b128 v251, v[120:123] offset:3072\n"
      "v_mfma_f32_16x16x32_bf16 v[152:155], a[48:51], v[200:203], 0\n"
      "ds_read_b64_tr_b16 a[200:201], v223 offset:49152\n"
      "ds_read_b64_tr_b16 a[202:203], v223 offset:51200\n"
      "v_mfma_f32_16x16x32_bf16 v[152:155], a[52:55], v[20:23], v[152:155]\n"
      "global_load_lds_dwordx4 v231, s[14:15] offset:0\n"
      "v_mfma_f32_16x16x32_bf16 v[152:155], a[56:59], v[68:71], v[152:155]\n"
      "v_cvt_pk_bf16_f32 v124, v124, v125\n"
      "v_cvt_pk_bf16_f32 v125, v126, v127\n"
      "v_mfma_f32_16x16x32_bf16 v[156:159], a[60:63], v[192:195], 0\n"
      "v_cvt_pk_bf16_f32 v126, v136, v137\n"
      "ds_read_b64_tr_b16 a[204:205], v223 offset:53248\n"
      "v_mfma_f32_16x16x32_bf16 v[156:159], a[64:67], v[12:15], v[156:159]\n"
      "v_cvt_pk_bf16_f32 v127, v138, v139\n"
      "ds_read_b64_tr_b16 a[206:207], v223 offset:55296\n"
      "v_mfma_f32_16x16x32_bf16 v[156:159], a[68:71], v[60:63], v[156:159]\n"
      "ds_write_b128 v251, v[124:127] offset:4096\n"
      "v_mfma_f32_16x16x32_bf16 v[160:163], a[68:71], v[64:67], 0\n"
      "v_cvt_pk_bf16_f32 v128, v128, v129\n"
      "ds_read_b64_tr_b16 a[208:209], v250 offset:49152\n"
      "v_mfma_f32_16x16x32_bf16 v[160:163], a[64:67], v[16:19], v[160:163]\n"
      "v_cvt_pk_bf16_f32 v129, v130, v131\n"
      "ds_read_b64_tr_b16 a[210:211], v250 offset:51200\n"
      "v_mfma_f32_16x16x32_bf16 v[160:163], a[60:63], v[196:199], v[160:163]\n"
      "v_cvt_pk_bf16_f32 v130, v140, v141\n"
      "ds_read_b64_tr_b16 a[212:213], v250 offset:53248\n"
      "v_mfma_f32_16x16x32_bf16 v[164:167], a[60:63], v[200:203], 0\n"
      "global_load_lds_dwordx4 v232, s[14:15] offset:1024\n"
      "v_mfma_f32_16x16x32_bf16 v[164:167], a[64:67], v[20:23], v[164:167]\n"
      "v_cvt_pk_bf16_f32 v131, v142, v143\n"
      "ds_read_b64_tr_b16 a[214:215], v250 offset:55296\n"
      "v_mfma_f32_16x16x32_bf16 v[164:167], a[68:71], v[68:71], v[164:167]\n"
      "ds_write_b128 v251, v[128:131] offset:5120\n"
      "v_mfma_f32_16x16x32_bf16 v[168:171], a[72:75], v[192:195], 0\n"
      "ds_read_b64_tr_b16 a[216:217], v224 offset:49152\n"
      "v_mfma_f32_16x16x32_bf16 v[168:171], a[76:79], v[12:15], v[168:171]\n"
      "ds_read_b64_tr_b16 a[218:219], v224 offset:51200\n"
      "ds_read_b64_tr_b16 a[220:221], v224 offset:53248\n"
      "v_mfma_f32_16x16x32_bf16 v[168:171], a[80:83], v[60:63], v[168:171]\n"
      "v_cvt_pk_bf16_f32 v144, v144, v145\n"
      "ds_read_b64_tr_b16 a[222:223], v224 offset:55296\n"
      "v_mfma_f32_16x16x32_bf16 v[172:175], a[80:83], v[64:67], 0\n"
      "v_cvt_pk_bf16_f32 v145, v146, v147\n"
      "v_cvt_pk_bf16_f32 v146, v156, v157\n"
      "v_mfma_f32_16x16x32_bf16 v[172:175], a[76:79], v[16:19], v[172:175]\n"
      "global_load_lds_dwordx4 v233, s[14:15] offset:2048\n"
      "v_mfma_f32_16x16x32_bf16 v[172:175], a[72:75], v[196:199], v[172:175]\n"
      "v_cvt_pk_bf16_f32 v147, v158, v159\n"
      "ds_read_b128 a[120:123], v252 offset:24576\n"
      "v_mfma_f32_16x16x32_bf16 v[176:179], a[72:75], v[200:203], 0\n"
      "ds_write_b128 v251, v[144:147] offset:24576\n"
      "v_mfma_f32_16x16x32_bf16 v[176:179], a[76:79], v[20:23], v[176:179]\n"
      "v_cvt_pk_bf16_f32 v148, v148, v149\n"
      "ds_read_b128 a[124:127], v252 offset:25600\n"
      "v_mfma_f32_16x16x32_bf16 v[176:179], a[80:83], v[68:71], v[176:179]\n"
      "v_cvt_pk_bf16_f32 v149, v150, v151\n"
      "ds_read_b128 a[128:131], v252 offset:26624\n"
      "v_mfma_f32_16x16x32_bf16 v[180:183], a[84:87], v[192:195], 0\n"
      "v_cvt_pk_bf16_f32 v150, v160, v161\n"
      "ds_read_b128 a[132:135], v252 offset:27648\n"
      "v_mfma_f32_16x16x32_bf16 v[180:183], a[88:91], v[12:15], v[180:183]\n"
      "v_cvt_pk_bf16_f32 v151, v162, v163\n"
      "ds_read_b128 a[136:139], v252 offset:28672\n"
      "v_mfma_f32_16x16x32_bf16 v[180:183], a[92:95], v[60:63], v[180:183]\n"
      "ds_write_b128 v251, v[148:151] offset:25600\n"
      "v_mfma_f32_16x16x32_bf16 v[184:187], a[92:95], v[64:67], 0\n"
      "global_load_lds_dwordx4 v234, s[14:15] offset:3072\n"
      "v_mfma_f32_16x16x32_bf16 v[184:187], a[88:91], v[16:19], v[184:187]\n"
      "v_cvt_pk_bf16_f32 v152, v152, v153\n"
      "ds_read_b128 a[140:143], v252 offset:29696\n"
      "v_mfma_f32_16x16x32_bf16 v[184:187], a[84:87], v[196:199], v[184:187]\n"
      "v_cvt_pk_bf16_f32 v153, v154, v155\n"
      "v_cvt_pk_bf16_f32 v154, v164, v165\n"
      "v_cvt_pk_bf16_f32 v155, v166, v167\n"
      "v_mfma_f32_16x16x32_bf16 v[188:191], a[84:87], v[200:203], 0\n"
      "ds_write_b128 v251, v[152:155] offset:26624\n"
      "v_mfma_f32_16x16x32_bf16 v[188:191], a[88:91], v[20:23], v[188:191]\n"
      "s_waitcnt vmcnt(8) lgkmcnt(1)\n"
      "v_mfma_f32_16x16x32_bf16 v[188:191], a[92:95], v[68:71], v[188:191]\n"
      "s_barrier\n"
      "v_mfma_f32_16x16x32_bf16 v[192:195], a[192:195], v[0:3], 0\n"
      "s_add_u32 s69, s69, 128\n"
      "ds_read_b64_tr_b16 a[224:225], v216 offset:49152\n"
      "v_mfma_f32_16x16x32_bf16 v[192:195], a[196:199], v[24:27], v[192:195]\n"
      "s_cmp_eq_u32 s69, 896\n"
      "s_cselect_b32 s69, 0, s69\n"
      "ds_read_b64_tr_b16 a[226:227], v216 offset:51200\n"
      "v_mfma_f32_16x16x32_bf16 v[192:195], a[200:203], a[96:99], v[192:195]\n"
      "v_cvt_pk_bf16_f32 v168, v168, v169\n"
      "v_cvt_pk_bf16_f32 v169, v170, v171\n"
      "ds_read_b64_tr_b16 a[228:229], v216 offset:53248\n"
      "v_mfma_f32_16x16x32_bf16 v[192:195], a[204:207], a[108:111], v[192:195]\n"
      "v_cvt_pk_bf16_f32 v170, v180, v181\n"
      "v_cvt_pk_bf16_f32 v171, v182, v183\n"
      "ds_read_b64_tr_b16 a[230:231], v216 offset:55296\n"
      "v_mfma_f32_16x16x32_bf16 v[200:203], a[204:207], a[112:115], 0\n"
      "ds_write_b128 v251, v[168:171] offset:27648\n"
      "v_mfma_f32_16x16x32_bf16 v[200:203], a[200:203], a[100:103], v[200:203]\n"
      "ds_read_b64_tr_b16 a[232:233], v218 offset:49152\n"
      "ds_read_b64_tr_b16 a[234:235], v218 offset:51200\n"
      "v_mfma_f32_16x16x32_bf16 v[200:203], a[196:199], v[28:31], v[200:203]\n"
      "ds_read_b64_tr_b16 a[236:237], v218 offset:53248\n"
      "ds_read_b64_tr_b16 a[238:239], v218 offset:55296\n"
      "v_mfma_f32_16x16x32_bf16 v[200:203], a[192:195], v[4:7], v[200:203]\n"
      "v_cvt_pk_bf16_f32 v172, v172, v173\n"
      "s_add_u32 s3, s69, s22\n"
      "s_add_u32 s14, s12, s3\n"
      "s_addc_u32 s15, s13, 0\n"
      "v_mfma_f32_16x16x32_bf16 v[208:211], a[192:195], v[8:11], 0\n"
      "v_cvt_pk_bf16_f32 v173, v174, v175\n"
      "s_add_u32 m0, s28, 0\n"
      "ds_read_b64_tr_b16 a[240:241], v217 offset:49152\n"
      "v_mfma_f32_16x16x32_bf16 v[208:211], a[196:199], v[32:35], v[208:211]\n"
      "global_load_lds_dwordx4 v231, s[14:15] offset:0\n"
      "v_mfma_f32_16x16x32_bf16 v[208:211], a[200:203], a[104:107], v[208:211]\n"
      "v_cvt_pk_bf16_f32 v174, v184, v185\n"
      "ds_read_b64_tr_b16 a[242:243], v217 offset:51200\n"
      "v_mfma_f32_16x16x32_bf16 v[208:211], a[204:207], a[116:119], v[208:211]\n"
      "v_cvt_pk_bf16_f32 v175, v186, v187\n"
      "ds_read_b64_tr_b16 a[244:245], v217 offset:53248\n"
      "v_mfma_f32_16x16x32_bf16 v[196:199], a[208:211], v[0:3], 0\n"
      "global_load_lds_dwordx4 v232, s[14:15] offset:1024\n"
      "v_mfma_f32_16x16x32_bf16 v[196:199], a[212:215], v[24:27], v[196:199]\n"
      "ds_write_b128 v251, v[172:175] offset:28672\n"
      "v_mfma_f32_16x16x32_bf16 v[196:199], a[216:219], a[96:99], v[196:199]\n"
      "v_cvt_pk_bf16_f32 v176, v176, v177\n"
      "v_cvt_pk_bf16_f32 v177, v178, v179\n"
      "v_cvt_pk_bf16_f32 v178, v188, v189\n"
      "v_mfma_f32_16x16x32_bf16 v[196:199], a[220:223], a[108:111], v[196:199]\n"
      "v_cvt_pk_bf16_f32 v179, v190, v191\n"
      "ds_read_b64_tr_b16 a[246:247], v217 offset:55296\n"
      "v_mfma_f32_16x16x32_bf16 v[204:207], a[220:223], a[112:115], 0\n"
      "ds_write_b128 v251, v[176:179] offset:29696\n"
      "v_mfma_f32_16x16x32_bf16 v[204:207], a[216:219], a[100:103], v[204:207]\n"
      "ds_read_b64_tr_b16 a[248:249], v219 offset:49152\n"
      "ds_read_b64_tr_b16 a[250:251], v219 offset:51200\n"
      "v_mfma_f32_16x16x32_bf16 v[204:207], a[212:215], v[28:31], v[204:207]\n"
      "global_load_lds_dwordx4 v233, s[14:15] offset:2048\n"
      "v_mfma_f32_16x16x32_bf16 v[204:207], a[208:211], v[4:7], v[204:207]\n"
      "global_load_lds_dwordx4 v234, s[14:15] offset:3072\n"
      "v_mfma_f32_16x16x32_bf16 v[212:215], a[208:211], v[8:11], 0\n"
      "ds_read_b64_tr_b16 a[252:253], v219 offset:53248\n"
      "ds_read_b64_tr_b16 a[254:255], v219 offset:55296\n"
      "v_mfma_f32_16x16x32_bf16 v[212:215], a[212:215], v[32:35], v[212:215]\n"
      "s_add_u32 s3, s69, s23\n"
      "s_add_u32 s14, s12, s3\n"
      "s_addc_u32 s15, s13, 0\n"
      "v_mfma_f32_16x16x32_bf16 v[212:215], a[216:219], a[104:107], v[212:215]\n"
      "s_waitcnt vmcnt(8) lgkmcnt(0)\n"
      "v_mfma_f32_16x16x32_bf16 v[212:215], a[220:223], a[116:119], v[212:215]\n"
      "s_barrier\n"
      "v_mfma_f32_16x16x32_bf16 v[192:195], a[224:227], v[48:51], v[192:195]\n"
      "s_add_u32 m0, s28, 16384\n"
      "ds_read_b64_tr_b16 a[192:193], v235 offset:0\n"
      "v_mfma_f32_16x16x32_bf16 v[192:195], a[228:231], v[72:75], v[192:195]\n"
      "ds_read_b64_tr_b16 a[194:195], v235 offset:2048\n"
      "ds_read_b64_tr_b16 a[196:197], v235 offset:4096\n"
      "v_mfma_f32_16x16x32_bf16 v[192:195], a[232:235], a[120:123], v[192:195]\n"
      "global_load_lds_dwordx4 v231, s[14:15] offset:0\n"
      "v_mfma_f32_16x16x32_bf16 v[192:195], a[236:239], a[132:135], v[192:195]\n"
      "ds_read_b64_tr_b16 a[198:199], v235 offset:6144\n"
      "ds_read_b64_tr_b16 a[200:201], v237 offset:0\n"
      "v_mfma_f32_16x16x32_bf16 v[200:203], a[236:239], a[136:139], v[200:203]\n"
      "global_load_lds_dwordx4 v232, s[14:15] offset:1024\n"
      "v_mfma_f32_16x16x32_bf16 v[200:203], a[232:235], a[124:127], v[200:203]\n"
      "ds_read_b64_tr_b16 a[202:203], v237 offset:2048\n"
      "ds_read_b64_tr_b16 a[204:205], v237 offset:4096\n"
      "v_mfma_f32_16x16x32_bf16 v[200:203], a[228:231], v[76:79], v[200:203]\n"
      "global_load_lds_dwordx4 v233, s[14:15] offset:2048\n"
      "v_mfma_f32_16x16x32_bf16 v[200:203], a[224:227], v[52:55], v[200:203]\n"
      "ds_read_b64_tr_b16 a[206:207], v237 offset:6144\n"
      "ds_read_b64_tr_b16 a[208:209], v236 offset:0\n"
      "v_mfma_f32_16x16x32_bf16 v[208:211], a[224:227], v[56:59], v[208:211]\n"
      "ds_read_b64_tr_b16 a[210:211], v236 offset:2048\n"
      "ds_read_b64_tr_b16 a[212:213], v236 offset:4096\n"
      "v_mfma_f32_16x16x32_bf16 v[208:211], a[228:231], v[80:83], v[208:211]\n"
      "global_load_lds_dwordx4 v234, s[14:15] offset:3072\n"
      "v_mfma_f32_16x16x32_bf16 v[208:211], a[232:235], a[128:131], v[208:211]\n"
      "s_add_u32 s3, s69, s34\n"
      "ds_read_b64_tr_b16 a[214:215], v236 offset:6144\n"
      "v_mfma_f32_16x16x32_bf16 v[208:211], a[236:239], a[140:143], v[208:211]\n"
      "ds_read_b64_tr_b16 a[216:217], v238 offset:0\n"
      "ds_read_b64_tr_b16 a[218:219], v238 offset:2048\n"
      "v_mfma_f32_16x16x32_bf16 v[196:199], a[240:243], v[48:51], v[196:199]\n"
      "s_add_u32 s14, s12, s3\n"
      "s_addc_u32 s15, s13, 0\n"
      "v_mfma_f32_16x16x32_bf16 v[196:199], a[244:247], v[72:75], v[196:199]\n"
      "ds_read_b64_tr_b16 a[220:221], v238 offset:4096\n"
      "ds_read_b64_tr_b16 a[222:223], v238 offset:6144\n"
      "v_mfma_f32_16x16x32_bf16 v[196:199], a[248:251], a[120:123], v[196:199]\n"
      "s_add_u32 m0, s28, 32768\n"
      "v_add_u32 v252, 0x10000, v252\n"
      "v_mfma_f32_16x16x32_bf16 v[196:199], a[252:255], a[132:135], v[196:199]\n"
      "ds_read_b128 a[144:147], v252 offset:0\n"
      "ds_read_b128 a[148:151], v252 offset:1024\n"
      "v_mfma_f32_16x16x32_bf16 v[204:207], a[252:255], a[136:139], v[204:207]\n"
      "global_load_lds_dwordx4 v231, s[14:15] offset:0\n"
      "v_mfma_f32_16x16x32_bf16 v[204:207], a[248:251], a[124:127], v[204:207]\n"
      "ds_read_b128 a[152:155], v252 offset:2048\n"
      "ds_read_b128 a[156:159], v252 offset:3072\n"
      "v_mfma_f32_16x16x32_bf16 v[204:207], a[244:247], v[76:79], v[204:207]\n"
      "global_load_lds_dwordx4 v232, s[14:15] offset:1024\n"
      "v_mfma_f32_16x16x32_bf16 v[204:207], a[240:243], v[52:55], v[204:207]\n"
      "ds_read_b128 a[160:163], v252 offset:4096\n"
      "ds_read_b128 a[164:167], v252 offset:5120\n"
      "v_mfma_f32_16x16x32_bf16 v[212:215], a[240:243], v[56:59], v[212:215]\n"
      "global_load_lds_dwordx4 v233, s[14:15] offset:2048\n"
      "v_mfma_f32_16x16x32_bf16 v[212:215], a[244:247], v[80:83], v[212:215]\n"
      "v_mfma_f32_16x16x32_bf16 v[212:215], a[248:251], a[128:131], v[212:215]\n"
      "s_waitcnt vmcnt(11) lgkmcnt(0)\n"
      "v_mfma_f32_16x16x32_bf16 v[212:215], a[252:255], a[140:143], v[212:215]\n"
      "s_barrier\n"
      "v_mfma_f32_16x16x32_bf16 v[192:195], a[192:195], v[96:99], v[192:195]\n"
      "ds_read_b64_tr_b16 a[224:225], v235 offset:16384\n"
      "ds_read_b64_tr_b16 a[226:227], v235 offset:18432\n"
      "v_mfma_f32_16x16x32_bf16 v[192:195], a[196:199], v[120:123], v[192:195]\n"
      "global_load_lds_dwordx4 v234, s[14:15] offset:3072\n"
      "v_mfma_f32_16x16x32_bf16 v[192:195], a[200:203], a[144:147], v[192:195]\n"
      "s_add_u32 s3, s69, s35\n"
      "ds_read_b64_tr_b16 a[228:229], v235 offset:20480\n"
      "v_mfma_f32_16x16x32_bf16 v[192:195], a[204:207], a[156:159], v[192:195]\n"
      "ds_read_b64_tr_b16 a[230:231], v235 offset:22528\n"
      "ds_read_b64_tr_b16 a[232:233], v237 offset:16384\n"
      "v_mfma_f32_16x16x32_bf16 v[200:203], a[204:207], a[160:163], v[200:203]\n"
      "s_add_u32 s14, s12, s3\n"
      "s_addc_u32 s15, s13, 0\n"
      "v_mfma_f32_16x16x32_bf16 v[200:203], a[200:203], a[148:151], v[200:203]\n"
      "ds_read_b64_tr_b16 a[234:235], v237 offset:18432\n"
      "ds_read_b64_tr_b16 a[236:237], v237 offset:20480\n"
      "v_mfma_f32_16x16x32_bf16 v[200:203], a[196:199], v[124:127], v[200:203]\n"
      "s_add_u32 m0, s28, 65536\n"
      "ds_read_b64_tr_b16 a[238:239], v237 offset:22528\n"
      "v_mfma_f32_16x16x32_bf16 v[200:203], a[192:195], v[100:103], v[200:203]\n"
      "global_load_lds_dwordx4 v231, s[14:15] offset:0\n"
      "v_mfma_f32_16x16x32_bf16 v[208:211], a[192:195], v[104:107], v[208:211]\n"
      "ds_read_b64_tr_b16 a[240:241], v236 offset:16384\n"
      "ds_read_b64_tr_b16 a[242:243], v236 offset:18432\n"
      "v_mfma_f32_16x16x32_bf16 v[208:211], a[196:199], v[128:131], v[208:211]\n"
      "global_load_lds_dwordx4 v232, s[14:15] offset:1024\n"
      "v_mfma_f32_16x16x32_bf16 v[208:211], a[200:203], a[152:155], v[208:211]\n"
      "ds_read_b64_tr_b16 a[244:245], v236 offset:20480\n"
      "ds_read_b64_tr_b16 a[246:247], v236 offset:22528\n"
      "v_mfma_f32_16x16x32_bf16 v[208:211], a[204:207], a[164:167], v[208:211]\n"
      "ds_read_b64_tr_b16 a[248:249], v238 offset:16384\n"
      "ds_read_b64_tr_b16 a[250:251], v238 offset:18432\n"
      "v_mfma_f32_16x16x32_bf16 v[196:199], a[208:211], v[96:99], v[196:199]\n"
      "global_load_lds_dwordx4 v233, s[14:15] offset:2048\n"
      "v_mfma_f32_16x16x32_bf16 v[196:199], a[212:215], v[120:123], v[196:199]\n"
      "ds_read_b64_tr_b16 a[252:253], v238 offset:20480\n"
      "ds_read_b64_tr_b16 a[254:255], v238 offset:22528\n"
      "v_mfma_f32_16x16x32_bf16 v[196:199], a[216:219], a[144:147], v[196:199]\n"
      "global_load_lds_dwordx4 v234, s[14:15] offset:3072\n"
      "v_mfma_f32_16x16x32_bf16 v[196:199], a[220:223], a[156:159], v[196:199]\n"
      "s_add_u32 s69, s69, 128\n"
      "ds_read_b128 a[168:171], v252 offset:24576\n"
      "v_mfma_f32_16x16x32_bf16 v[204:207], a[220:223], a[160:163], v[204:207]\n"
      "ds_read_b128 a[172:175], v252 offset:25600\n"
      "ds_read_b128 a[176:179], v252 offset:26624\n"
      "v_mfma_f32_16x16x32_bf16 v[204:207], a[216:219], a[148:151], v[204:207]\n"
      "s_cmp_eq_u32 s69, 896\n"
      "s_cselect_b32 s69, 0, s69\n"
      "v_mfma_f32_16x16x32_bf16 v[204:207], a[212:215], v[124:127], v[204:207]\n"
      "ds_read_b128 a[180:183], v252 offset:27648\n"
      "ds_read_b128 a[184:187], v252 offset:28672\n"
      "v_mfma_f32_16x16x32_bf16 v[204:207], a[208:211], v[100:103], v[204:207]\n"
      "s_add_u32 s3, s69, s22\n"
      "ds_read_b128 a[188:191], v252 offset:29696\n"
      "v_mfma_f32_16x16x32_bf16 v[212:215], a[208:211], v[104:107], v[212:215]\n"
      "s_add_u32 s14, s12, s3\n"
      "s_addc_u32 s15, s13, 0\n"
      "v_mfma_f32_16x16x32_bf16 v[212:215], a[212:215], v[128:131], v[212:215]\n"
      "v_mfma_f32_16x16x32_bf16 v[212:215], a[216:219], a[152:155], v[212:215]\n"
      "s_waitcnt vmcnt(12) lgkmcnt(0)\n"
      "v_mfma_f32_16x16x32_bf16 v[212:215], a[220:223], a[164:167], v[212:215]\n"
      "s_barrier\n"
      "v_mfma_f32_16x16x32_bf16 v[192:195], a[224:227], v[144:147], v[192:195]\n"
      "s_add_u32 m0, s28, 81920\n"
      "ds_read_b64_tr_b16 a[192:193], v249 offset:0\n"
      "v_mfma_f32_16x16x32_bf16 v[192:195], a[228:231], v[168:171], v[192:195]\n"
      "global_load_lds_dwordx4 v231, s[14:15] offset:0\n"
      "v_mfma_f32_16x16x32_bf16 v[192:195], a[232:235], a[168:171], v[192:195]\n"
      "ds_read_b64_tr_b16 a[194:195], v249 offset:2048\n"
      "ds_read_b64_tr_b16 a[196:197], v249 offset:4096\n"
      "v_mfma_f32_16x16x32_bf16 v[192:195], a[236:239], a[180:183], v[192:195]\n"
      "global_load_lds_dwordx4 v232, s[14:15] offset:1024\n"
      "v_mfma_f32_16x16x32_bf16 v[196:199], a[252:255], a[180:183], v[196:199]\n"
      "ds_read_b64_tr_b16 a[198:199], v249 offset:6144\n"
      "v_mfma_f32_16x16x32_bf16 v[196:199], a[248:251], a[168:171], v[196:199]\n"
      "global_load_lds_dwordx4 v233, s[14:15] offset:2048\n"
      "v_mfma_f32_16x16x32_bf16 v[196:199], a[244:247], v[168:171], v[196:199]\n"
      "ds_read_b64_tr_b16 a[200:201], v223 offset:0\n"
      "ds_read_b64_tr_b16 a[202:203], v223 offset:2048\n"
      "v_mfma_f32_16x16x32_bf16 v[196:199], a[240:243], v[144:147], v[196:199]\n"
      "ds_read_b64_tr_b16 a[204:205], v223 offset:4096\n"
      "ds_read_b64_tr_b16 a[206:207], v223 offset:6144\n"
      "v_mfma_f32_16x16x32_bf16 v[200:203], a[224:227], v[148:151], v[200:203]\n"
      "v_cvt_pk_bf16_f32 v12, v192, v193\n"
      "v_cvt_pk_bf16_f32 v13, v194, v195\n"
      "v_mfma_f32_16x16x32_bf16 v[200:203], a[228:231], v[172:175], v[200:203]\n"
      "global_load_lds_dwordx4 v234, s[14:15] offset:3072\n"
      "v_mfma_f32_16x16x32_bf16 v[200:203], a[232:235], a[172:175], v[200:203]\n"
      "s_add_u32 s3, s69, s23\n"
      "s_add_u32 s14, s12, s3\n"
      "s_addc_u32 s15, s13, 0\n"
      "v_mfma_f32_16x16x32_bf16 v[200:203], a[236:239], a[184:187], v[200:203]\n"
      "v_bitop3_b32 v247, v12, v13, s47 bitop3:0x96\n"
      "ds_read_b64_tr_b16 a[208:209], v250 offset:0\n"
      "v_mfma_f32_16x16x32_bf16 v[204:207], a[252:255], a[184:187], v[204:207]\n"
      "v_cvt_pk_bf16_f32 v14, v196, v197\n"
      "ds_read_b64_tr_b16 a[210:211], v250 offset:2048\n"
      "v_mfma_f32_16x16x32_bf16 v[204:207], a[248:251], a[172:175], v[204:207]\n"
      "v_cvt_pk_bf16_f32 v15, v198, v199\n"
      "ds_read_b64_tr_b16 a[212:213], v250 offset:4096\n"
      "v_mfma_f32_16x16x32_bf16 v[204:207], a[244:247], v[172:175], v[204:207]\n"
      "ds_read_b64_tr_b16 a[214:215], v250 offset:6144\n"
      "ds_read_b64_tr_b16 a[216:217], v224 offset:0\n"
      "v_mfma_f32_16x16x32_bf16 v[204:207], a[240:243], v[148:151], v[204:207]\n"
      "v_bitop3_b32 v247, v14, v15, v247 bitop3:0x96\n"
      "ds_read_b64_tr_b16 a[218:219], v224 offset:2048\n"
      "v_mfma_f32_16x16x32_bf16 v[208:211], a[224:227], v[152:155], v[208:211]\n"
      "s_add_u32 m0, s28, 98304\n"
      "ds_read_b64_tr_b16 a[220:221], v224 offset:4096\n"
      "v_mfma_f32_16x16x32_bf16 v[208:211], a[228:231], v[176:179], v[208:211]\n"
      "global_load_lds_dwordx4 v231, s[14:15] offset:0\n"
      "v_mfma_f32_16x16x32_bf16 v[208:211], a[232:235], a[176:179], v[208:211]\n"
      "v_cvt_pk_bf16_f32 v16, v200, v201\n"
      "ds_read_b64_tr_b16 a[222:223], v224 offset:6144\n"
      "v_mfma_f32_16x16x32_bf16 v[208:211], a[236:239], a[188:191], v[208:211]\n"
      "v_cvt_pk_bf16_f32 v17, v202, v203\n"
      "v_bitop3_b32 v247, v16, v17, v247 bitop3:0x96\n"
      "v_mfma_f32_16x16x32_bf16 v[212:215], a[252:255], a[188:191], v[212:215]\n"
      "v_cvt_pk_bf16_f32 v18, v204, v205\n"
      "v_cvt_pk_bf16_f32 v19, v206, v207\n"
      "v_mfma_f32_16x16x32_bf16 v[212:215], a[248:251], a[176:179], v[212:215]\n"
      "global_load_lds_dwordx4 v232, s[14:15] offset:1024\n"
      "v_mfma_f32_16x16x32_bf16 v[212:215], a[244:247], v[176:179], v[212:215]\n"
      "s_waitcnt vmcnt(14) lgkmcnt(0)\n"
      "v_mfma_f32_16x16x32_bf16 v[212:215], a[240:243], v[152:155], v[212:215]\n"
      "v_bitop3_b32 v247, v18, v19, v247 bitop3:0x96\n"
      "s_barrier\n"
      "v_mfma_f32_16x16x32_bf16 v[192:195], a[192:195], v[0:3], 0\n"
      "ds_read_b64_tr_b16 a[224:225], v249 offset:16384\n"
      "ds_read_b64_tr_b16 a[226:227], v249 offset:18432\n"
      "v_mfma_f32_16x16x32_bf16 v[192:195], a[196:199], v[24:27], v[192:195]\n"
      "v_cvt_pk_bf16_f32 v20, v208, v209\n"
      "v_cvt_pk_bf16_f32 v21, v210, v211\n"
      "ds_read_b64_tr_b16 a[228:229], v249 offset:20480\n"
      "v_mfma_f32_16x16x32_bf16 v[192:195], a[200:203], a[96:99], v[192:195]\n"
      "v_bitop3_b32 v247, v20, v21, v247 bitop3:0x96\n"
      "v_cvt_pk_bf16_f32 v22, v212, v213\n"
      "ds_read_b64_tr_b16 a[230:231], v249 offset:22528\n"
      "v_mfma_f32_16x16x32_bf16 v[192:195], a[204:207], a[108:111], v[192:195]\n"
      "v_cvt_pk_bf16_f32 v23, v214, v215\n"
      "v_bitop3_b32 v247, v22, v23, v247 bitop3:0x96\n"
      "ds_read_b64_tr_b16 a[232:233], v223 offset:16384\n"
      "v_mfma_f32_16x16x32_bf16 v[200:203], a[204:207], a[112:115], 0\n"
      "buffer_store_dwordx4 v[12:15], v253, s[24:27], s68 offen offset:0 sc1\n"
      "v_mfma_f32_16x16x32_bf16 v[200:203], a[200:203], a[100:103], v[200:203]\n"
      "global_load_lds_dwordx4 v233, s[14:15] offset:2048\n"
      "v_mfma_f32_16x16x32_bf16 v[200:203], a[196:199], v[28:31], v[200:203]\n"
      "buffer_store_dwordx4 v[16:19], v253, s[24:27], s68 offen offset:1024 sc1\n"
      "v_mfma_f32_16x16x32_bf16 v[200:203], a[192:195], v[4:7], v[200:203]\n"
      "ds_read_b64_tr_b16 a[234:235], v223 offset:18432\n"
      "ds_read_b64_tr_b16 a[236:237], v223 offset:20480\n"
      "ds_read_b64_tr_b16 a[238:239], v223 offset:22528\n"
      "v_mfma_f32_16x16x32_bf16 v[208:211], a[192:195], v[8:11], 0\n"
      "buffer_store_dwordx4 v[20:23], v253, s[24:27], s68 offen offset:2048 sc1\n"
      "v_mfma_f32_16x16x32_bf16 v[208:211], a[196:199], v[32:35], v[208:211]\n"
      "global_load_lds_dwordx4 v234, s[14:15] offset:3072\n"
      "v_mfma_f32_16x16x32_bf16 v[208:211], a[200:203], a[104:107], v[208:211]\n"
      "buffer_store_dword v247, v221, s[24:27], s68 offen offset:3072 sc1\n"
      "v_mfma_f32_16x16x32_bf16 v[208:211], a[204:207], a[116:119], v[208:211]\n"
      "ds_read_b64_tr_b16 a[240:241], v250 offset:16384\n"
      "ds_read_b64_tr_b16 a[242:243], v250 offset:18432\n"
      "v_mfma_f32_16x16x32_bf16 v[196:199], a[208:211], v[0:3], 0\n"
      "v_mov_b32 v252, s37\n"
      "ds_read_b64_tr_b16 a[244:245], v250 offset:20480\n"
      "v_mfma_f32_16x16x32_bf16 v[196:199], a[212:215], v[24:27], v[196:199]\n"
      "buffer_store_dword v252, off, s[24:27], s36 offset:0 sc1\n"
      "v_mfma_f32_16x16x32_bf16 v[196:199], a[216:219], a[96:99], v[196:199]\n"
      "s_add_u32 s3, s69, s34\n"
      "ds_read_b64_tr_b16 a[246:247], v250 offset:22528\n"
      "v_mfma_f32_16x16x32_bf16 v[196:199], a[220:223], a[108:111], v[196:199]\n"
      "s_add_u32 s37, s37, 1\n"
      "ds_read_b64_tr_b16 a[248:249], v224 offset:16384\n"
      "v_mfma_f32_16x16x32_bf16 v[204:207], a[220:223], a[112:115], 0\n"
      "ds_read_b64_tr_b16 a[250:251], v224 offset:18432\n"
      "ds_read_b64_tr_b16 a[252:253], v224 offset:20480\n"
      "v_mfma_f32_16x16x32_bf16 v[204:207], a[216:219], a[100:103], v[204:207]\n"
      "s_add_u32 s68, s68, 13312\n"
      "ds_read_b64_tr_b16 a[254:255], v224 offset:22528\n"
      "v_mfma_f32_16x16x32_bf16 v[204:207], a[212:215], v[28:31], v[204:207]\n"
      "s_add_u32 s14, s12, s3\n"
      "s_addc_u32 s15, s13, 0\n"
      "v_mfma_f32_16x16x32_bf16 v[204:207], a[208:211], v[4:7], v[204:207]\n"
      "s_sub_u32 s49, s68, 93184\n"
      "s_add_u32 m0, s28, 49152\n"
      "v_mfma_f32_16x16x32_bf16 v[212:215], a[208:211], v[8:11], 0\n"
      "global_load_lds_dwordx4 v231, s[14:15] offset:0\n"
      "v_mfma_f32_16x16x32_bf16 v[212:215], a[212:215], v[32:35], v[212:215]\n"
      "s_cmp_eq_u32 s68, s79\n"
      "s_cselect_b32 s68, s49, s68\n"
      "v_mfma_f32_16x16x32_bf16 v[212:215], a[216:219], a[104:107], v[212:215]\n"
      "s_waitcnt vmcnt(18) lgkmcnt(0)\n"
      "v_mfma_f32_16x16x32_bf16 v[212:215], a[220:223], a[116:119], v[212:215]\n"
      "s_barrier\n"
      "v_mfma_f32_16x16x32_bf16 v[192:195], a[224:227], v[48:51], v[192:195]\n"
      "ds_read_b64_tr_b16 a[192:193], v249 offset:32768\n"
      "v_mfma_f32_16x16x32_bf16 v[192:195], a[228:231], v[72:75], v[192:195]\n"
      "ds_read_b64_tr_b16 a[194:195], v249 offset:34816\n"
      "v_mfma_f32_16x16x32_bf16 v[192:195], a[232:235], a[120:123], v[192:195]\n"
      "global_load_lds_dwordx4 v232, s[14:15] offset:1024\n"
      "v_mfma_f32_16x16x32_bf16 v[192:195], a[236:239], a[132:135], v[192:195]\n"
      "ds_read_b64_tr_b16 a[196:197], v249 offset:36864\n"
      "v_mfma_f32_16x16x32_bf16 v[200:203], a[236:239], a[136:139], v[200:203]\n"
      "ds_read_b64_tr_b16 a[198:199], v249 offset:38912\n"
      "v_mfma_f32_16x16x32_bf16 v[200:203], a[232:235], a[124:127], v[200:203]\n"
      "ds_read_b64_tr_b16 a[200:201], v223 offset:32768\n"
      "v_mfma_f32_16x16x32_bf16 v[200:203], a[228:231], v[76:79], v[200:203]\n"
      "ds_read_b64_tr_b16 a[202:203], v223 offset:34816\n"
      "v_mfma_f32_16x16x32_bf16 v[200:203], a[224:227], v[52:55], v[200:203]\n"
      "global_load_lds_dwordx4 v233, s[14:15] offset:2048\n"
      "v_mfma_f32_16x16x32_bf16 v[208:211], a[224:227], v[56:59], v[208:211]\n"
      "ds_read_b64_tr_b16 a[204:205], v223 offset:36864\n"
      "v_mfma_f32_16x16x32_bf16 v[208:211], a[228:231], v[80:83], v[208:211]\n"
      "ds_read_b64_tr_b16 a[206:207], v223 offset:38912\n"
      "v_mfma_f32_16x16x32_bf16 v[208:211], a[232:235], a[128:131], v[208:211]\n"
      "ds_read_b64_tr_b16 a[208:209], v250 offset:32768\n"
      "v_mfma_f32_16x16x32_bf16 v[208:211], a[236:239], a[140:143], v[208:211]\n"
      "ds_read_b64_tr_b16 a[210:211], v250 offset:34816\n"
      "v_mfma_f32_16x16x32_bf16 v[196:199], a[240:243], v[48:51], v[196:199]\n"
      "global_load_lds_dwordx4 v234, s[14:15] offset:3072\n"
      "v_mfma_f32_16x16x32_bf16 v[196:199], a[244:247], v[72:75], v[196:199]\n"
      "ds_read_b64_tr_b16 a[212:213], v250 offset:36864\n"
      "v_mfma_f32_16x16x32_bf16 v[196:199], a[248:251], a[120:123], v[196:199]\n"
      "ds_read_b64_tr_b16 a[214:215], v250 offset:38912\n"
      "v_mfma_f32_16x16x32_bf16 v[196:199], a[252:255], a[132:135], v[196:199]\n"
      "s_add_u32 s3, s69, s35\n"
      "ds_read_b64_tr_b16 a[216:217], v224 offset:32768\n"
      "v_mfma_f32_16x16x32_bf16 v[204:207], a[252:255], a[136:139], v[204:207]\n"
      "ds_read_b64_tr_b16 a[218:219], v224 offset:34816\n"
      "v_mfma_f32_16x16x32_bf16 v[204:207], a[248:251], a[124:127], v[204:207]\n"
      "ds_read_b64_tr_b16 a[220:221], v224 offset:36864\n"
      "v_mfma_f32_16x16x32_bf16 v[204:207], a[244:247], v[76:79], v[204:207]\n"
      "ds_read_b64_tr_b16 a[222:223], v224 offset:38912\n"
      "v_mfma_f32_16x16x32_bf16 v[204:207], a[240:243], v[52:55], v[204:207]\n"
      "s_add_u32 s14, s12, s3\n"
      "s_addc_u32 s15, s13, 0\n"
      "v_mfma_f32_16x16x32_bf16 v[212:215], a[240:243], v[56:59], v[212:215]\n"
      "v_mfma_f32_16x16x32_bf16 v[212:215], a[244:247], v[80:83], v[212:215]\n"
      "v_mfma_f32_16x16x32_bf16 v[212:215], a[248:251], a[128:131], v[212:215]\n"
      "s_waitcnt vmcnt(17) lgkmcnt(0)\n"
      "v_mfma_f32_16x16x32_bf16 v[212:215], a[252:255], a[140:143], v[212:215]\n"
      "s_barrier\n"
      "v_mfma_f32_16x16x32_bf16 v[192:195], a[192:195], v[96:99], v[192:195]\n"
      "s_add_u32 m0, s28, 114688\n"
      "ds_read_b64_tr_b16 a[224:225], v216 offset:0\n"
      "v_mfma_f32_16x16x32_bf16 v[192:195], a[196:199], v[120:123], v[192:195]\n"
      "global_load_lds_dwordx4 v231, s[14:15] offset:0\n"
      "v_mfma_f32_16x16x32_bf16 v[192:195], a[200:203], a[144:147], v[192:195]\n"
      "ds_read_b64_tr_b16 a[226:227], v216 offset:2048\n"
      "ds_read_b64_tr_b16 a[228:229], v216 offset:4096\n"
      "v_mfma_f32_16x16x32_bf16 v[192:195], a[204:207], a[156:159], v[192:195]\n"
      "global_load_lds_dwordx4 v232, s[14:15] offset:1024\n"
      "v_mfma_f32_16x16x32_bf16 v[200:203], a[204:207], a[160:163], v[200:203]\n"
      "ds_read_b64_tr_b16 a[230:231], v216 offset:6144\n"
      "v_mfma_f32_16x16x32_bf16 v[200:203], a[200:203], a[148:151], v[200:203]\n"
      "global_load_lds_dwordx4 v233, s[14:15] offset:2048\n"
      "v_mfma_f32_16x16x32_bf16 v[200:203], a[196:199], v[124:127], v[200:203]\n"
      "ds_read_b64_tr_b16 a[232:233], v218 offset:0\n"
      "ds_read_b64_tr_b16 a[234:235], v218 offset:2048\n"
      "v_mfma_f32_16x16x32_bf16 v[200:203], a[192:195], v[100:103], v[200:203]\n"
      "global_load_lds_dwordx4 v234, s[14:15] offset:3072\n"
      "v_mfma_f32_16x16x32_bf16 v[208:211], a[192:195], v[104:107], v[208:211]\n"
      "ds_read_b64_tr_b16 a[236:237], v218 offset:4096\n"
      "ds_read_b64_tr_b16 a[238:239], v218 offset:6144\n"
      "v_mfma_f32_16x16x32_bf16 v[208:211], a[196:199], v[128:131], v[208:211]\n"
      "s_add_u32 s69, s69, 128\n"
      "ds_read_b64_tr_b16 a[240:241], v217 offset:0\n"
      "v_mfma_f32_16x16x32_bf16 v[208:211], a[200:203], a[152:155], v[208:211]\n"
      "s_cmp_eq_u32 s69, 896\n"
      "s_cselect_b32 s69, 0, s69\n"
      "v_mfma_f32_16x16x32_bf16 v[208:211], a[204:207], a[164:167], v[208:211]\n"
      "ds_read_b64_tr_b16 a[242:243], v217 offset:2048\n"
      "ds_read_b64_tr_b16 a[244:245], v217 offset:4096\n"
      "v_mfma_f32_16x16x32_bf16 v[196:199], a[208:211], v[96:99], v[196:199]\n"
      "s_add_u32 s3, s69, s22\n"
      "ds_read_b64_tr_b16 a[246:247], v217 offset:6144\n"
      "v_mfma_f32_16x16x32_bf16 v[196:199], a[212:215], v[120:123], v[196:199]\n"
      "s_add_u32 s14, s12, s3\n"
      "s_addc_u32 s15, s13, 0\n"
      "v_mfma_f32_16x16x32_bf16 v[196:199], a[216:219], a[144:147], v[196:199]\n"
      "ds_read_b64_tr_b16 a[248:249], v219 offset:0\n"
      "v_mfma_f32_16x16x32_bf16 v[196:199], a[220:223], a[156:159], v[196:199]\n"
      "s_add_u32 m0, s28, 131072\n"
      "ds_read_b64_tr_b16 a[250:251], v219 offset:2048\n"
      "v_mfma_f32_16x16x32_bf16 v[204:207], a[220:223], a[160:163], v[204:207]\n"
      "global_load_lds_dwordx4 v231, s[14:15] offset:0\n"
      "v_mfma_f32_16x16x32_bf16 v[204:207], a[216:219], a[148:151], v[204:207]\n"
      "ds_read_b64_tr_b16 a[252:253], v219 offset:4096\n"
      "ds_read_b64_tr_b16 a[254:255], v219 offset:6144\n"
      "v_mfma_f32_16x16x32_bf16 v[204:207], a[212:215], v[124:127], v[204:207]\n"
      "global_load_lds_dwordx4 v232, s[14:15] offset:1024\n"
      "v_mfma_f32_16x16x32_bf16 v[204:207], a[208:211], v[100:103], v[204:207]\n"
      "v_mfma_f32_16x16x32_bf16 v[212:215], a[208:211], v[104:107], v[212:215]\n"
      "global_load_lds_dwordx4 v233, s[14:15] offset:2048\n"
      "v_mfma_f32_16x16x32_bf16 v[212:215], a[212:215], v[128:131], v[212:215]\n"
      "v_mfma_f32_16x16x32_bf16 v[212:215], a[216:219], a[152:155], v[212:215]\n"
      "s_waitcnt vmcnt(20) lgkmcnt(0)\n"
      "v_mfma_f32_16x16x32_bf16 v[212:215], a[220:223], a[164:167], v[212:215]\n"
      "s_barrier\n"
      "v_mfma_f32_16x16x32_bf16 v[192:195], a[224:227], v[144:147], v[192:195]\n"
      "global_load_lds_dwordx4 v234, s[14:15] offset:3072\n"
      "v_mfma_f32_16x16x32_bf16 v[192:195], a[228:231], v[168:171], v[192:195]\n"
      "s_add_u32 s3, s69, s23\n"
      "s_add_u32 s14, s12, s3\n"
      "s_addc_u32 s15, s13, 0\n"
      "ds_read_b64_tr_b16 a[192:193], v216 offset:16384\n"
      "v_mfma_f32_16x16x32_bf16 v[192:195], a[232:235], a[168:171], v[192:195]\n"
      "ds_read_b64_tr_b16 a[194:195], v216 offset:18432\n"
      "ds_read_b64_tr_b16 a[196:197], v216 offset:20480\n"
      "ds_read_b64_tr_b16 a[198:199], v216 offset:22528\n"
      "v_mfma_f32_16x16x32_bf16 v[192:195], a[236:239], a[180:183], v[192:195]\n"
      "ds_read_b64_tr_b16 a[200:201], v218 offset:16384\n"
      "ds_read_b64_tr_b16 a[202:203], v218 offset:18432\n"
      "v_mfma_f32_16x16x32_bf16 v[196:199], a[252:255], a[180:183], v[196:199]\n"
      "s_add_u32 m0, s28, 147456\n"
      "ds_read_b64_tr_b16 a[204:205], v218 offset:20480\n"
      "v_mfma_f32_16x16x32_bf16 v[196:199], a[248:251], a[168:171], v[196:199]\n"
      "global_load_lds_dwordx4 v231, s[14:15] offset:0\n"
      "v_mfma_f32_16x16x32_bf16 v[196:199], a[244:247], v[168:171], v[196:199]\n"
      "global_load_lds_dwordx4 v232, s[14:15] offset:1024\n"
      "v_mfma_f32_16x16x32_bf16 v[196:199], a[240:243], v[144:147], v[196:199]\n"
      "v_cvt_pk_bf16_f32 v12, v192, v193\n"
      "v_cvt_pk_bf16_f32 v13, v194, v195\n"
      "v_mfma_f32_16x16x32_bf16 v[200:203], a[224:227], v[148:151], v[200:203]\n"
      "v_bitop3_b32 v247, v12, v13, s47 bitop3:0x96\n"
      "ds_read_b64_tr_b16 a[206:207], v218 offset:22528\n"
      "v_mfma_f32_16x16x32_bf16 v[200:203], a[228:231], v[172:175], v[200:203]\n"
      "global_load_lds_dwordx4 v233, s[14:15] offset:2048\n"
      "v_mfma_f32_16x16x32_bf16 v[200:203], a[232:235], a[172:175], v[200:203]\n"
      "global_load_lds_dwordx4 v234, s[14:15] offset:3072\n"
      "v_mfma_f32_16x16x32_bf16 v[200:203], a[236:239], a[184:187], v[200:203]\n"
      "s_add_u32 s3, s69, s34\n"
      "ds_read_b64_tr_b16 a[208:209], v217 offset:16384\n"
      "v_mfma_f32_16x16x32_bf16 v[204:207], a[252:255], a[184:187], v[204:207]\n"
      "s_add_u32 s14, s12, s3\n"
      "s_addc_u32 s15, s13, 0\n"
      "ds_read_b64_tr_b16 a[210:211], v217 offset:18432\n"
      "v_mfma_f32_16x16x32_bf16 v[204:207], a[248:251], a[172:175], v[204:207]\n"
      "ds_read_b64_tr_b16 a[212:213], v217 offset:20480\n"
      "ds_read_b64_tr_b16 a[214:215], v217 offset:22528\n"
      "v_mfma_f32_16x16x32_bf16 v[204:207], a[244:247], v[172:175], v[204:207]\n"
      "v_cvt_pk_bf16_f32 v14, v196, v197\n"
      "ds_read_b64_tr_b16 a[216:217], v219 offset:16384\n"
      "v_mfma_f32_16x16x32_bf16 v[204:207], a[240:243], v[148:151], v[204:207]\n"
      "s_add_u32 m0, s28, 0\n"
      "ds_read_b64_tr_b16 a[218:219], v219 offset:18432\n"
      "v_mfma_f32_16x16x32_bf16 v[208:211], a[224:227], v[152:155], v[208:211]\n"
      "v_cvt_pk_bf16_f32 v15, v198, v199\n"
      "ds_read_b64_tr_b16 a[220:221], v219 offset:20480\n"
      "v_mfma_f32_16x16x32_bf16 v[208:211], a[228:231], v[176:179], v[208:211]\n"
      "v_bitop3_b32 v247, v14, v15, v247 bitop3:0x96\n"
      "ds_read_b64_tr_b16 a[222:223], v219 offset:22528\n"
      "v_mfma_f32_16x16x32_bf16 v[208:211], a[232:235], a[176:179], v[208:211]\n"
      "global_load_lds_dwordx4 v231, s[14:15] offset:0\n"
      "v_mfma_f32_16x16x32_bf16 v[208:211], a[236:239], a[188:191], v[208:211]\n"
      "v_cvt_pk_bf16_f32 v16, v200, v201\n"
      "v_cvt_pk_bf16_f32 v17, v202, v203\n"
      "v_mfma_f32_16x16x32_bf16 v[212:215], a[252:255], a[188:191], v[212:215]\n"
      "v_bitop3_b32 v247, v16, v17, v247 bitop3:0x96\n"
      "v_cvt_pk_bf16_f32 v18, v204, v205\n"
      "v_mfma_f32_16x16x32_bf16 v[212:215], a[248:251], a[176:179], v[212:215]\n"
      "global_load_lds_dwordx4 v232, s[14:15] offset:1024\n"
      "v_mfma_f32_16x16x32_bf16 v[212:215], a[244:247], v[176:179], v[212:215]\n"
      "v_cvt_pk_bf16_f32 v19, v206, v207\n"
      "s_waitcnt vmcnt(20) lgkmcnt(0)\n"
      "v_mfma_f32_16x16x32_bf16 v[212:215], a[240:243], v[152:155], v[212:215]\n"
      "v_bitop3_b32 v247, v18, v19, v247 bitop3:0x96\n"
      "s_barrier\n"
      "v_mfma_f32_16x16x32_bf16 v[192:195], a[192:195], v[0:3], 0\n"
      "ds_read_b64_tr_b16 a[224:225], v216 offset:32768\n"
      "ds_read_b64_tr_b16 a[226:227], v216 offset:34816\n"
      "v_mfma_f32_16x16x32_bf16 v[192:195], a[196:199], v[24:27], v[192:195]\n"
      "v_cvt_pk_bf16_f32 v20, v208, v209\n"
      "v_cvt_pk_bf16_f32 v21, v210, v211\n"
      "v_mfma_f32_16x16x32_bf16 v[192:195], a[200:203], a[96:99], v[192:195]\n"
      "v_bitop3_b32 v247, v20, v21, v247 bitop3:0x96\n"
      "v_cvt_pk_bf16_f32 v22, v212, v213\n"
      "v_mfma_f32_16x16x32_bf16 v[192:195], a[204:207], a[108:111], v[192:195]\n"
      "v_cvt_pk_bf16_f32 v23, v214, v215\n"
      "v_bitop3_b32 v247, v22, v23, v247 bitop3:0x96\n"
      "v_mfma_f32_16x16x32_bf16 v[200:203], a[204:207], a[112:115], 0\n"
      "buffer_store_dwordx4 v[12:15], v253, s[24:27], s68 offen offset:0 sc1\n"
      "v_mfma_f32_16x16x32_bf16 v[200:203], a[200:203], a[100:103], v[200:203]\n"
      "global_load_lds_dwordx4 v233, s[14:15] offset:2048\n"
      "v_mfma_f32_16x16x32_bf16 v[200:203], a[196:199], v[28:31], v[200:203]\n"
      "buffer_store_dwordx4 v[16:19], v253, s[24:27], s68 offen offset:1024 sc1\n"
      "v_mfma_f32_16x16x32_bf16 v[200:203], a[192:195], v[4:7], v[200:203]\n"
      "ds_read_b64_tr_b16 a[228:229], v216 offset:36864\n"
      "ds_read_b64_tr_b16 a[230:231], v216 offset:38912\n"
      "v_mfma_f32_16x16x32_bf16 v[208:211], a[192:195], v[8:11], 0\n"
      "buffer_store_dwordx4 v[20:23], v253, s[24:27], s68 offen offset:2048 sc1\n"
      "v_mfma_f32_16x16x32_bf16 v[208:211], a[196:199], v[32:35], v[208:211]\n"
      "ds_read_b64_tr_b16 a[232:233], v218 offset:32768\n"
      "ds_read_b64_tr_b16 a[234:235], v218 offset:34816\n"
      "ds_read_b64_tr_b16 a[236:237], v218 offset:36864\n"
      "v_mfma_f32_16x16x32_bf16 v[208:211], a[200:203], a[104:107], v[208:211]\n"
      "buffer_store_dword v247, v221, s[24:27], s68 offen offset:3072 sc1\n"
      "v_mfma_f32_16x16x32_bf16 v[208:211], a[204:207], a[116:119], v[208:211]\n"
      "global_load_lds_dwordx4 v234, s[14:15] offset:3072\n"
      "v_mfma_f32_16x16x32_bf16 v[196:199], a[208:211], v[0:3], 0\n"
      "v_mov_b32 v252, s37\n"
      "ds_read_b64_tr_b16 a[238:239], v218 offset:38912\n"
      "v_mfma_f32_16x16x32_bf16 v[196:199], a[212:215], v[24:27], v[196:199]\n"
      "buffer_store_dword v252, off, s[24:27], s36 offset:0 sc1\n"
      "v_mfma_f32_16x16x32_bf16 v[196:199], a[216:219], a[96:99], v[196:199]\n"
      "ds_read_b64_tr_b16 a[240:241], v217 offset:32768\n"
      "ds_read_b64_tr_b16 a[242:243], v217 offset:34816\n"
      "v_mfma_f32_16x16x32_bf16 v[196:199], a[220:223], a[108:111], v[196:199]\n"
      "s_add_u32 s37, s37, 1\n"
      "ds_read_b64_tr_b16 a[244:245], v217 offset:36864\n"
      "v_mfma_f32_16x16x32_bf16 v[204:207], a[220:223], a[112:115], 0\n"
      "ds_read_b64_tr_b16 a[246:247], v217 offset:38912\n"
      "ds_read_b64_tr_b16 a[248:249], v219 offset:32768\n"
      "v_mfma_f32_16x16x32_bf16 v[204:207], a[216:219], a[100:103], v[204:207]\n"
      "s_add_u32 s3, s69, s35\n"
      "ds_read_b64_tr_b16 a[250:251], v219 offset:34816\n"
      "v_mfma_f32_16x16x32_bf16 v[204:207], a[212:215], v[28:31], v[204:207]\n"
      "ds_read_b64_tr_b16 a[252:253], v219 offset:36864\n"
      "ds_read_b64_tr_b16 a[254:255], v219 offset:38912\n"
      "v_mfma_f32_16x16x32_bf16 v[204:207], a[208:211], v[4:7], v[204:207]\n"
      "s_add_u32 s68, s68, 13312\n"
      "s_sub_u32 s49, s68, 93184\n"
      "v_mfma_f32_16x16x32_bf16 v[212:215], a[208:211], v[8:11], 0\n"
      "s_add_u32 s14, s12, s3\n"
      "s_addc_u32 s15, s13, 0\n"
      "v_mfma_f32_16x16x32_bf16 v[212:215], a[212:215], v[32:35], v[212:215]\n"
      "s_cmp_eq_u32 s68, s79\n"
      "s_cselect_b32 s68, s49, s68\n"
      "v_mfma_f32_16x16x32_bf16 v[212:215], a[216:219], a[104:107], v[212:215]\n"
      "s_waitcnt vmcnt(21) lgkmcnt(0)\n"
      "v_mfma_f32_16x16x32_bf16 v[212:215], a[220:223], a[116:119], v[212:215]\n"
      "s_barrier\n"
      "v_mfma_f32_16x16x32_bf16 v[192:195], a[224:227], v[48:51], v[192:195]\n"
      "ds_read_b64_tr_b16 a[192:193], v249 offset:49152\n"
      "v_bfe_u32 v240, v239, 2, 2\n"
      "v_mfma_f32_16x16x32_bf16 v[192:195], a[228:231], v[72:75], v[192:195]\n"
      "s_add_u32 m0, s28, 16384\n"
      "ds_read_b64_tr_b16 a[194:195], v249 offset:51200\n"
      "v_mfma_f32_16x16x32_bf16 v[192:195], a[232:235], a[120:123], v[192:195]\n"
      "ds_read_b64_tr_b16 a[196:197], v249 offset:53248\n"
      "v_lshrrev_b32 v241, 4, v239\n"
      "v_mfma_f32_16x16x32_bf16 v[192:195], a[236:239], a[132:135], v[192:195]\n"
      "ds_read_b64_tr_b16 a[198:199], v249 offset:55296\n"
      "v_and_b32 v244, 1, v239\n"
      "v_mfma_f32_16x16x32_bf16 v[200:203], a[236:239], a[136:139], v[200:203]\n"
      "ds_read_b64_tr_b16 a[200:201], v223 offset:49152\n"
      "v_lshlrev_b32 v244, 4, v244\n"
      "v_mfma_f32_16x16x32_bf16 v[200:203], a[232:235], a[124:127], v[200:203]\n"
      "ds_read_b64_tr_b16 a[202:203], v223 offset:51200\n"
      "v_mov_b32 v243, 0x3f80\n"
      "v_mfma_f32_16x16x32_bf16 v[200:203], a[228:231], v[76:79], v[200:203]\n"
      "ds_read_b64_tr_b16 a[204:205], v223 offset:53248\n"
      "v_lshlrev_b32 v244, v244, v243\n"
      "v_mfma_f32_16x16x32_bf16 v[200:203], a[224:227], v[52:55], v[200:203]\n"
      "global_load_lds_dwordx4 v231, s[14:15] offset:0\n"
      "v_mfma_f32_16x16x32_bf16 v[208:211], a[224:227], v[56:59], v[208:211]\n"
      "ds_read_b64_tr_b16 a[206:207], v223 offset:55296\n"
      "v_bfe_u32 v245, v239, 1, 1\n"
      "v_mfma_f32_16x16x32_bf16 v[208:211], a[228:231], v[80:83], v[208:211]\n"
      "ds_read_b64_tr_b16 a[208:209], v250 offset:49152\n"
      "ds_read_b64_tr_b16 a[210:211], v250 offset:51200\n"
      "v_mfma_f32_16x16x32_bf16 v[208:211], a[232:235], a[128:131], v[208:211]\n"
      "ds_read_b64_tr_b16 a[212:213], v250 offset:53248\n"
      "v_lshl_add_u32 v240, v245, 2, v240\n"
      "v_mfma_f32_16x16x32_bf16 v[208:211], a[236:239], a[140:143], v[208:211]\n"
      "ds_read_b64_tr_b16 a[214:215], v250 offset:55296\n"
      "v_mfma_f32_16x16x32_bf16 v[196:199], a[240:243], v[48:51], v[196:199]\n"
      "ds_read_b64_tr_b16 a[216:217], v224 offset:49152\n"
      "v_mfma_f32_16x16x32_bf16 v[196:199], a[244:247], v[72:75], v[196:199]\n"
      "ds_read_b64_tr_b16 a[218:219], v224 offset:51200\n"
      "v_mfma_f32_16x16x32_bf16 v[196:199], a[248:251], a[120:123], v[196:199]\n"
      "ds_read_b64_tr_b16 a[220:221], v224 offset:53248\n"
      "v_mfma_f32_16x16x32_bf16 v[196:199], a[252:255], a[132:135], v[196:199]\n"
      "global_load_lds_dwordx4 v232, s[14:15] offset:1024\n"
      "v_mfma_f32_16x16x32_bf16 v[204:207], a[252:255], a[136:139], v[204:207]\n"
      "s_add_u32 s4, s60, 1\n"
      "s_add_u32 s5, s61, 2\n"
      "v_mfma_f32_16x16x32_bf16 v[204:207], a[248:251], a[124:127], v[204:207]\n"
      "s_add_u32 s18, s62, 3\n"
      "s_add_u32 s19, s63, 4\n"
      "v_mfma_f32_16x16x32_bf16 v[204:207], a[244:247], v[76:79], v[204:207]\n"
      "ds_read_b64_tr_b16 a[222:223], v224 offset:55296\n"
      "v_mfma_f32_16x16x32_bf16 v[204:207], a[240:243], v[52:55], v[204:207]\n"
      "v_mfma_f32_16x16x32_bf16 v[212:215], a[240:243], v[56:59], v[212:215]\n"
      "v_mfma_f32_16x16x32_bf16 v[212:215], a[244:247], v[80:83], v[212:215]\n"
      "v_mfma_f32_16x16x32_bf16 v[212:215], a[248:251], a[128:131], v[212:215]\n"
      "s_waitcnt vmcnt(19) lgkmcnt(0)\n"
      "v_mfma_f32_16x16x32_bf16 v[212:215], a[252:255], a[140:143], v[212:215]\n"
      "s_barrier\n"
      "v_mfma_f32_16x16x32_bf16 v[192:195], a[192:195], v[96:99], v[192:195]\n"
      "global_load_lds_dwordx4 v233, s[14:15] offset:2048\n"
      "v_mfma_f32_16x16x32_bf16 v[192:195], a[196:199], v[120:123], v[192:195]\n"
      "ds_read_b64_tr_b16 a[224:225], v216 offset:49152\n"
      "ds_read_b64_tr_b16 a[226:227], v216 offset:51200\n"
      "v_mfma_f32_16x16x32_bf16 v[192:195], a[200:203], a[144:147], v[192:195]\n"
      "global_load_lds_dwordx4 v234, s[14:15] offset:3072\n"
      "v_mfma_f32_16x16x32_bf16 v[192:195], a[204:207], a[156:159], v[192:195]\n"
      "v_cmp_eq_u32 vcc, v240, v241\n"
      "ds_read_b64_tr_b16 a[228:229], v216 offset:53248\n"
      "v_mfma_f32_16x16x32_bf16 v[200:203], a[204:207], a[160:163], v[200:203]\n"
      "s_add_u32 s69, s69, 128\n"
      "ds_read_b64_tr_b16 a[230:231], v216 offset:55296\n"
      "v_mfma_f32_16x16x32_bf16 v[200:203], a[200:203], a[148:151], v[200:203]\n"
      "s_cmp_eq_u32 s69, 896\n"
      "s_cselect_b32 s69, 0, s69\n"
      "v_mfma_f32_16x16x32_bf16 v[200:203], a[196:199], v[124:127], v[200:203]\n"
      "ds_read_b64_tr_b16 a[232:233], v218 offset:49152\n"
      "ds_read_b64_tr_b16 a[234:235], v218 offset:51200\n"
      "v_mfma_f32_16x16x32_bf16 v[200:203], a[192:195], v[100:103], v[200:203]\n"
      "s_add_u32 s3, s69, s22\n"
      "ds_read_b64_tr_b16 a[236:237], v218 offset:53248\n"
      "v_mfma_f32_16x16x32_bf16 v[208:211], a[192:195], v[104:107], v[208:211]\n"
      "s_add_u32 s14, s12, s3\n"
      "s_addc_u32 s15, s13, 0\n"
      "v_mfma_f32_16x16x32_bf16 v[208:211], a[196:199], v[128:131], v[208:211]\n"
      "v_add_u32 v241, 4, v241\n"
      "ds_read_b64_tr_b16 a[238:239], v218 offset:55296\n"
      "v_mfma_f32_16x16x32_bf16 v[208:211], a[200:203], a[152:155], v[208:211]\n"
      "s_add_u32 m0, s28, 32768\n"
      "ds_read_b64_tr_b16 a[240:241], v217 offset:49152\n"
      "v_mfma_f32_16x16x32_bf16 v[208:211], a[204:207], a[164:167], v[208:211]\n"
      "global_load_lds_dwordx4 v231, s[14:15] offset:0\n"
      "v_mfma_f32_16x16x32_bf16 v[196:199], a[208:211], v[96:99], v[196:199]\n"
      "ds_read_b64_tr_b16 a[242:243], v217 offset:51200\n"
      "ds_read_b64_tr_b16 a[244:245], v217 offset:53248\n"
      "v_mfma_f32_16x16x32_bf16 v[196:199], a[212:215], v[120:123], v[196:199]\n"
      "global_load_lds_dwordx4 v232, s[14:15] offset:1024\n"
      "v_mfma_f32_16x16x32_bf16 v[196:199], a[216:219], a[144:147], v[196:199]\n"
      "ds_read_b64_tr_b16 a[246:247], v217 offset:55296\n"
      "ds_read_b64_tr_b16 a[248:249], v219 offset:49152\n"
      "v_mfma_f32_16x16x32_bf16 v[196:199], a[220:223], a[156:159], v[196:199]\n"
      "global_load_lds_dwordx4 v233, s[14:15] offset:2048\n"
      "v_mfma_f32_16x16x32_bf16 v[204:207], a[220:223], a[160:163], v[204:207]\n"
      "v_mov_b32 v245, 0\n"
      "ds_read_b64_tr_b16 a[250:251], v219 offset:51200\n"
      "v_mfma_f32_16x16x32_bf16 v[204:207], a[216:219], a[148:151], v[204:207]\n"
      "global_load_lds_dwordx4 v234, s[14:15] offset:3072\n"
      "v_mfma_f32_16x16x32_bf16 v[204:207], a[212:215], v[124:127], v[204:207]\n"
      "ds_read_b64_tr_b16 a[252:253], v219 offset:53248\n"
      "ds_read_b64_tr_b16 a[254:255], v219 offset:55296\n"
      "v_mfma_f32_16x16x32_bf16 v[204:207], a[208:211], v[100:103], v[204:207]\n"
      "v_cndmask_b32 v242, 0, v244, vcc\n"
      "s_add_u32 s3, s69, s23\n"
      "v_mfma_f32_16x16x32_bf16 v[212:215], a[208:211], v[104:107], v[212:215]\n"
      "s_add_u32 s14, s12, s3\n"
      "s_addc_u32 s15, s13, 0\n"
      "v_mfma_f32_16x16x32_bf16 v[212:215], a[212:215], v[128:131], v[212:215]\n"
      "v_cmp_eq_u32 vcc, v240, v241\n"
      "v_mov_b32 v240, 0\n"
      "v_mfma_f32_16x16x32_bf16 v[212:215], a[216:219], a[152:155], v[212:215]\n"
      "v_mov_b32 v241, 0\n"
      "s_waitcnt vmcnt(21) lgkmcnt(0)\n"
      "v_mfma_f32_16x16x32_bf16 v[212:215], a[220:223], a[164:167], v[212:215]\n"
      "v_cndmask_b32 v243, 0, v244, vcc\n"
      "v_mov_b32 v244, 0\n"
      "s_barrier\n"
      "v_mfma_f32_16x16x32_bf16 v[192:195], a[224:227], v[144:147], v[192:195]\n"
      "s_add_u32 m0, s28, 65536\n"
      "ds_read_b64_tr_b16 a[192:193], v235 offset:0\n"
      "v_mfma_f32_16x16x32_bf16 v[192:195], a[228:231], v[168:171], v[192:195]\n"
      "global_load_lds_dwordx4 v231, s[14:15] offset:0\n"
      "v_mfma_f32_16x16x32_bf16 v[192:195], a[232:235], a[168:171], v[192:195]\n"
      "ds_read_b64_tr_b16 a[194:195], v235 offset:2048\n"
      "ds_read_b64_tr_b16 a[196:197], v235 offset:4096\n"
      "v_mfma_f32_16x16x32_bf16 v[192:195], a[236:239], a[180:183], v[192:195]\n"
      "global_load_lds_dwordx4 v232, s[14:15] offset:1024\n"
      "v_mfma_f32_16x16x32_bf16 v[196:199], a[252:255], a[180:183], v[196:199]\n"
      "ds_read_b64_tr_b16 a[198:199], v235 offset:6144\n"
      "v_mfma_f32_16x16x32_bf16 v[196:199], a[248:251], a[168:171], v[196:199]\n"
      "global_load_lds_dwordx4 v233, s[14:15] offset:2048\n"
      "v_mfma_f32_16x16x32_bf16 v[196:199], a[244:247], v[168:171], v[196:199]\n"
      "ds_read_b64_tr_b16 a[200:201], v237 offset:0\n"
      "ds_read_b64_tr_b16 a[202:203], v237 offset:2048\n"
      "v_mfma_f32_16x16x32_bf16 v[196:199], a[240:243], v[144:147], v[196:199]\n"
      "ds_read_b64_tr_b16 a[204:205], v237 offset:4096\n"
      "ds_read_b64_tr_b16 a[206:207], v237 offset:6144\n"
      "v_mfma_f32_16x16x32_bf16 v[200:203], a[224:227], v[148:151], v[200:203]\n"
      "v_cvt_pk_bf16_f32 v12, v192, v193\n"
      "v_cvt_pk_bf16_f32 v13, v194, v195\n"
      "v_mfma_f32_16x16x32_bf16 v[200:203], a[228:231], v[172:175], v[200:203]\n"
      "global_load_lds_dwordx4 v234, s[14:15] offset:3072\n"
      "v_mfma_f32_16x16x32_bf16 v[200:203], a[232:235], a[172:175], v[200:203]\n"
      "s_add_u32 s3, s69, s34\n"
      "s_add_u32 s14, s12, s3\n"
      "s_addc_u32 s15, s13, 0\n"
      "v_mfma_f32_16x16x32_bf16 v[200:203], a[236:239], a[184:187], v[200:203]\n"
      "v_bitop3_b32 v247, v12, v13, s47 bitop3:0x96\n"
      "ds_read_b64_tr_b16 a[208:209], v236 offset:0\n"
      "v_mfma_f32_16x16x32_bf16 v[204:207], a[252:255], a[184:187], v[204:207]\n"
      "v_cvt_pk_bf16_f32 v14, v196, v197\n"
      "ds_read_b64_tr_b16 a[210:211], v236 offset:2048\n"
      "v_mfma_f32_16x16x32_bf16 v[204:207], a[248:251], a[172:175], v[204:207]\n"
      "v_cvt_pk_bf16_f32 v15, v198, v199\n"
      "ds_read_b64_tr_b16 a[212:213], v236 offset:4096\n"
      "v_mfma_f32_16x16x32_bf16 v[204:207], a[244:247], v[172:175], v[204:207]\n"
      "ds_read_b64_tr_b16 a[214:215], v236 offset:6144\n"
      "ds_read_b64_tr_b16 a[216:217], v238 offset:0\n"
      "v_mfma_f32_16x16x32_bf16 v[204:207], a[240:243], v[148:151], v[204:207]\n"
      "v_bitop3_b32 v247, v14, v15, v247 bitop3:0x96\n"
      "ds_read_b64_tr_b16 a[218:219], v238 offset:2048\n"
      "v_mfma_f32_16x16x32_bf16 v[208:211], a[224:227], v[152:155], v[208:211]\n"
      "s_add_u32 m0, s28, 81920\n"
      "ds_read_b64_tr_b16 a[220:221], v238 offset:4096\n"
      "v_mfma_f32_16x16x32_bf16 v[208:211], a[228:231], v[176:179], v[208:211]\n"
      "global_load_lds_dwordx4 v231, s[14:15] offset:0\n"
      "v_mfma_f32_16x16x32_bf16 v[208:211], a[232:235], a[176:179], v[208:211]\n"
      "v_cvt_pk_bf16_f32 v16, v200, v201\n"
      "ds_read_b64_tr_b16 a[222:223], v238 offset:6144\n"
      "v_mfma_f32_16x16x32_bf16 v[208:211], a[236:239], a[188:191], v[208:211]\n"
      "v_cvt_pk_bf16_f32 v17, v202, v203\n"
      "v_bitop3_b32 v247, v16, v17, v247 bitop3:0x96\n"
      "v_mfma_f32_16x16x32_bf16 v[212:215], a[252:255], a[188:191], v[212:215]\n"
      "v_cvt_pk_bf16_f32 v18, v204, v205\n"
      "v_cvt_pk_bf16_f32 v19, v206, v207\n"
      "v_mfma_f32_16x16x32_bf16 v[212:215], a[248:251], a[176:179], v[212:215]\n"
      "global_load_lds_dwordx4 v232, s[14:15] offset:1024\n"
      "v_mfma_f32_16x16x32_bf16 v[212:215], a[244:247], v[176:179], v[212:215]\n"
      "s_waitcnt vmcnt(23) lgkmcnt(0)\n"
      "v_mfma_f32_16x16x32_bf16 v[212:215], a[240:243], v[152:155], v[212:215]\n"
      "v_bitop3_b32 v247, v18, v19, v247 bitop3:0x96\n"
      "s_barrier\n"
      "v_mfma_f32_16x16x32_bf16 v[192:195], a[192:195], v[0:3], 0\n"
      "ds_read_b64_tr_b16 a[224:225], v235 offset:16384\n"
      "ds_read_b64_tr_b16 a[226:227], v235 offset:18432\n"
      "v_mfma_f32_16x16x32_bf16 v[192:195], a[196:199], v[24:27], v[192:195]\n"
      "v_cvt_pk_bf16_f32 v20, v208, v209\n"
      "v_cvt_pk_bf16_f32 v21, v210, v211\n"
      "v_mfma_f32_16x16x32_bf16 v[192:195], a[200:203], a[96:99], v[192:195]\n"
      "v_bitop3_b32 v247, v20, v21, v247 bitop3:0x96\n"
      "v_cvt_pk_bf16_f32 v22, v212, v213\n"
      "ds_read_b64_tr_b16 a[228:229], v235 offset:20480\n"
      "v_mfma_f32_16x16x32_bf16 v[192:195], a[204:207], a[108:111], v[192:195]\n"
      "v_cvt_pk_bf16_f32 v23, v214, v215\n"
      "v_bitop3_b32 v247, v22, v23, v247 bitop3:0x96\n"
      "v_mfma_f32_16x16x32_bf16 v[200:203], a[204:207], a[112:115], 0\n"
      "buffer_store_dwordx4 v[12:15], v253, s[24:27], s68 offen offset:0 sc1\n"
      "v_mfma_f32_16x16x32_bf16 v[200:203], a[200:203], a[100:103], v[200:203]\n"
      "ds_read_b64_tr_b16 a[230:231], v235 offset:22528\n"
      "ds_read_b64_tr_b16 a[232:233], v237 offset:16384\n"
      "v_mfma_f32_16x16x32_bf16 v[200:203], a[196:199], v[28:31], v[200:203]\n"
      "buffer_store_dwordx4 v[16:19], v253, s[24:27], s68 offen offset:1024 sc1\n"
      "v_mfma_f32_16x16x32_bf16 v[200:203], a[192:195], v[4:7], v[200:203]\n"
      "ds_read_b64_tr_b16 a[234:235], v237 offset:18432\n"
      "ds_read_b64_tr_b16 a[236:237], v237 offset:20480\n"
      "ds_read_b64_tr_b16 a[238:239], v237 offset:22528\n"
      "v_mfma_f32_16x16x32_bf16 v[208:211], a[192:195], v[8:11], 0\n"
      "buffer_store_dwordx4 v[20:23], v253, s[24:27], s68 offen offset:2048 sc1\n"
      "v_mfma_f32_16x16x32_bf16 v[208:211], a[196:199], v[32:35], v[208:211]\n"
      "ds_read_b64_tr_b16 a[240:241], v236 offset:16384\n"
      "ds_read_b64_tr_b16 a[242:243], v236 offset:18432\n"
      "v_mfma_f32_16x16x32_bf16 v[208:211], a[200:203], a[104:107], v[208:211]\n"
      "buffer_store_dword v247, v221, s[24:27], s68 offen offset:3072 sc1\n"
      "v_mfma_f32_16x16x32_bf16 v[208:211], a[204:207], a[116:119], v[208:211]\n"
      "ds_read_b64_tr_b16 a[244:245], v236 offset:20480\n"
      "ds_read_b64_tr_b16 a[246:247], v236 offset:22528\n"
      "v_mfma_f32_16x16x32_bf16 v[196:199], a[208:211], v[0:3], 0\n"
      "v_mov_b32 v252, s37\n"
      "ds_read_b64_tr_b16 a[248:249], v238 offset:16384\n"
      "v_mfma_f32_16x16x32_bf16 v[196:199], a[212:215], v[24:27], v[196:199]\n"
      "buffer_store_dword v252, off, s[24:27], s36 offset:0 sc1\n"
      "v_mfma_f32_16x16x32_bf16 v[196:199], a[216:219], a[96:99], v[196:199]\n"
      "global_load_lds_dwordx4 v233, s[14:15] offset:2048\n"
      "v_mfma_f32_16x16x32_bf16 v[196:199], a[220:223], a[108:111], v[196:199]\n"
      "s_add_u32 s37, s37, 1\n"
      "ds_read_b64_tr_b16 a[250:251], v238 offset:18432\n"
      "v_mfma_f32_16x16x32_bf16 v[204:207], a[220:223], a[112:115], 0\n"
      "s_add_u32 s68, s68, 13312\n"
      "ds_read_b64_tr_b16 a[252:253], v238 offset:20480\n"
      "v_mfma_f32_16x16x32_bf16 v[204:207], a[216:219], a[100:103], v[204:207]\n"
      "s_sub_u32 s49, s68, 93184\n"
      "ds_read_b64_tr_b16 a[254:255], v238 offset:22528\n"
      "v_mfma_f32_16x16x32_bf16 v[204:207], a[212:215], v[28:31], v[204:207]\n"
      "s_cmp_eq_u32 s68, s79\n"
      "s_cselect_b32 s68, s49, s68\n"
      "v_mfma_f32_16x16x32_bf16 v[204:207], a[208:211], v[4:7], v[204:207]\n"
      "v_mfma_f32_16x16x32_bf16 v[212:215], a[208:211], v[8:11], 0\n"
      "v_mfma_f32_16x16x32_bf16 v[212:215], a[212:215], v[32:35], v[212:215]\n"
      "v_mfma_f32_16x16x32_bf16 v[212:215], a[216:219], a[104:107], v[212:215]\n"
      "s_waitcnt vmcnt(21) lgkmcnt(0)\n"
      "v_mfma_f32_16x16x32_bf16 v[212:215], a[220:223], a[116:119], v[212:215]\n"
      "s_barrier\n"
      "v_mfma_f32_16x16x32_bf16 v[192:195], a[224:227], v[48:51], v[192:195]\n"
      "ds_read_b64_tr_b16 a[192:193], v249 offset:0\n"
      "v_mfma_f32_16x16x32_bf16 v[192:195], a[228:231], v[72:75], v[192:195]\n"
      "ds_read_b64_tr_b16 a[194:195], v249 offset:2048\n"
      "v_mfma_f32_16x16x32_bf16 v[192:195], a[232:235], a[120:123], v[192:195]\n"
      "global_load_lds_dwordx4 v234, s[14:15] offset:3072\n"
      "v_mfma_f32_16x16x32_bf16 v[192:195], a[236:239], a[132:135], v[192:195]\n"
      "ds_read_b64_tr_b16 a[196:197], v249 offset:4096\n"
      "v_mfma_f32_16x16x32_bf16 v[200:203], a[236:239], a[136:139], v[200:203]\n"
      "ds_read_b64_tr_b16 a[198:199], v249 offset:6144\n"
      "v_mfma_f32_16x16x32_bf16 v[200:203], a[232:235], a[124:127], v[200:203]\n"
      "ds_read_b64_tr_b16 a[200:201], v223 offset:0\n"
      "v_mfma_f32_16x16x32_bf16 v[200:203], a[228:231], v[76:79], v[200:203]\n"
      "s_add_u32 s3, s69, s35\n"
      "ds_read_b64_tr_b16 a[202:203], v223 offset:2048\n"
      "v_mfma_f32_16x16x32_bf16 v[200:203], a[224:227], v[52:55], v[200:203]\n"
      "ds_read_b64_tr_b16 a[204:205], v223 offset:4096\n"
      "v_mfma_f32_16x16x32_bf16 v[208:211], a[224:227], v[56:59], v[208:211]\n"
      "ds_read_b64_tr_b16 a[206:207], v223 offset:6144\n"
      "v_mfma_f32_16x16x32_bf16 v[208:211], a[228:231], v[80:83], v[208:211]\n"
      "v_mfma_f32_16x16x32_bf16 v[208:211], a[232:235], a[128:131], v[208:211]\n"
      "s_add_u32 s14, s12, s3\n"
      "s_addc_u32 s15, s13, 0\n"
      "v_mfma_f32_16x16x32_bf16 v[208:211], a[236:239], a[140:143], v[208:211]\n"
      "ds_read_b64_tr_b16 a[208:209], v250 offset:0\n"
      "v_mfma_f32_16x16x32_bf16 v[196:199], a[240:243], v[48:51], v[196:199]\n"
      "ds_read_b64_tr_b16 a[210:211], v250 offset:2048\n"
      "v_mfma_f32_16x16x32_bf16 v[196:199], a[244:247], v[72:75], v[196:199]\n"
      "ds_read_b64_tr_b16 a[212:213], v250 offset:4096\n"
      "v_mfma_f32_16x16x32_bf16 v[196:199], a[248:251], a[120:123], v[196:199]\n"
      "ds_read_b64_tr_b16 a[214:215], v250 offset:6144\n"
      "v_mfma_f32_16x16x32_bf16 v[196:199], a[252:255], a[132:135], v[196:199]\n"
      "s_add_u32 m0, s28, 98304\n"
      "ds_read_b64_tr_b16 a[216:217], v224 offset:0\n"
      "v_mfma_f32_16x16x32_bf16 v[204:207], a[252:255], a[136:139], v[204:207]\n"
      "ds_read_b64_tr_b16 a[218:219], v224 offset:2048\n"
      "v_mfma_f32_16x16x32_bf16 v[204:207], a[248:251], a[124:127], v[204:207]\n"
      "ds_read_b64_tr_b16 a[220:221], v224 offset:4096\n"
      "v_mfma_f32_16x16x32_bf16 v[204:207], a[244:247], v[76:79], v[204:207]\n"
      "ds_read_b64_tr_b16 a[222:223], v224 offset:6144\n"
      "v_mfma_f32_16x16x32_bf16 v[204:207], a[240:243], v[52:55], v[204:207]\n"
      "global_load_lds_dwordx4 v231, s[14:15] offset:0\n"
      "v_mfma_f32_16x16x32_bf16 v[212:215], a[240:243], v[56:59], v[212:215]\n"
      "v_mfma_f32_16x16x32_bf16 v[212:215], a[244:247], v[80:83], v[212:215]\n"
      "v_mfma_f32_16x16x32_bf16 v[212:215], a[248:251], a[128:131], v[212:215]\n"
      "s_waitcnt vmcnt(18) lgkmcnt(0)\n"
      "v_mfma_f32_16x16x32_bf16 v[212:215], a[252:255], a[140:143], v[212:215]\n"
      "s_barrier\n"
      "v_mfma_f32_16x16x32_bf16 v[192:195], a[192:195], v[96:99], v[192:195]\n"
      "ds_read_b64_tr_b16 a[224:225], v249 offset:16384\n"
      "v_mfma_f32_16x16x32_bf16 v[192:195], a[196:199], v[120:123], v[192:195]\n"
      "global_load_lds_dwordx4 v232, s[14:15] offset:1024\n"
      "v_mfma_f32_16x16x32_bf16 v[192:195], a[200:203], a[144:147], v[192:195]\n"
      "ds_read_b64_tr_b16 a[226:227], v249 offset:18432\n"
      "ds_read_b64_tr_b16 a[228:229], v249 offset:20480\n"
      "v_mfma_f32_16x16x32_bf16 v[192:195], a[204:207], a[156:159], v[192:195]\n"
      "global_load_lds_dwordx4 v233, s[14:15] offset:2048\n"
      "v_mfma_f32_16x16x32_bf16 v[200:203], a[204:207], a[160:163], v[200:203]\n"
      "ds_read_b64_tr_b16 a[230:231], v249 offset:22528\n"
      "v_mfma_f32_16x16x32_bf16 v[200:203], a[200:203], a[148:151], v[200:203]\n"
      "ds_read_b64_tr_b16 a[232:233], v223 offset:16384\n"
      "v_mfma_f32_16x16x32_bf16 v[200:203], a[196:199], v[124:127], v[200:203]\n"
      "global_load_lds_dwordx4 v234, s[14:15] offset:3072\n"
      "v_mfma_f32_16x16x32_bf16 v[200:203], a[192:195], v[100:103], v[200:203]\n"
      "ds_read_b64_tr_b16 a[234:235], v223 offset:18432\n"
      "v_mfma_f32_16x16x32_bf16 v[208:211], a[192:195], v[104:107], v[208:211]\n"
      "s_add_u32 s69, s69, 128\n"
      "ds_read_b64_tr_b16 a[236:237], v223 offset:20480\n"
      "v_mfma_f32_16x16x32_bf16 v[208:211], a[196:199], v[128:131], v[208:211]\n"
      "ds_read_b64_tr_b16 a[238:239], v223 offset:22528\n"
      "ds_read_b64_tr_b16 a[240:241], v250 offset:16384\n"
      "v_mfma_f32_16x16x32_bf16 v[208:211], a[200:203], a[152:155], v[208:211]\n"
      "s_cmp_eq_u32 s69, 896\n"
      "s_cselect_b32 s69, 0, s69\n"
      "v_mfma_f32_16x16x32_bf16 v[208:211], a[204:207], a[164:167], v[208:211]\n"
      "ds_read_b64_tr_b16 a[242:243], v250 offset:18432\n"
      "v_mfma_f32_16x16x32_bf16 v[196:199], a[208:211], v[96:99], v[196:199]\n"
      "s_add_u32 s3, s69, s22\n"
      "ds_read_b64_tr_b16 a[244:245], v250 offset:20480\n"
      "v_mfma_f32_16x16x32_bf16 v[196:199], a[212:215], v[120:123], v[196:199]\n"
      "ds_read_b64_tr_b16 a[246:247], v250 offset:22528\n"
      "v_mfma_f32_16x16x32_bf16 v[196:199], a[216:219], a[144:147], v[196:199]\n"
      "s_add_u32 s14, s12, s3\n"
      "s_addc_u32 s15, s13, 0\n"
      "v_mfma_f32_16x16x32_bf16 v[196:199], a[220:223], a[156:159], v[196:199]\n"
      "ds_read_b64_tr_b16 a[248:249], v224 offset:16384\n"
      "ds_read_b64_tr_b16 a[250:251], v224 offset:18432\n"
      "v_mfma_f32_16x16x32_bf16 v[204:207], a[220:223], a[160:163], v[204:207]\n"
      "s_add_u32 m0, s28, 49152\n"
      "ds_read_b64_tr_b16 a[252:253], v224 offset:20480\n"
      "v_mfma_f32_16x16x32_bf16 v[204:207], a[216:219], a[148:151], v[204:207]\n"
      "ds_read_b64_tr_b16 a[254:255], v224 offset:22528\n"
      "v_mfma_f32_16x16x32_bf16 v[204:207], a[212:215], v[124:127], v[204:207]\n"
      "global_load_lds_dwordx4 v231, s[14:15] offset:0\n"
      "v_mfma_f32_16x16x32_bf16 v[204:207], a[208:211], v[100:103], v[204:207]\n"
      "v_mfma_f32_16x16x32_bf16 v[212:215], a[208:211], v[104:107], v[212:215]\n"
      "global_load_lds_dwordx4 v232, s[14:15] offset:1024\n"
      "v_mfma_f32_16x16x32_bf16 v[212:215], a[212:215], v[128:131], v[212:215]\n"
      "v_mfma_f32_16x16x32_bf16 v[212:215], a[216:219], a[152:155], v[212:215]\n"
      "s_waitcnt vmcnt(19) lgkmcnt(0)\n"
      "v_mfma_f32_16x16x32_bf16 v[212:215], a[220:223], a[164:167], v[212:215]\n"
      "s_barrier\n"
      "v_mfma_f32_16x16x32_bf16 v[192:195], a[224:227], v[144:147], v[192:195]\n"
      "ds_read_b64_tr_b16 a[192:193], v249 offset:32768\n"
      "ds_read_b64_tr_b16 a[194:195], v249 offset:34816\n"
      "v_mfma_f32_16x16x32_bf16 v[192:195], a[228:231], v[168:171], v[192:195]\n"
      "global_load_lds_dwordx4 v233, s[14:15] offset:2048\n"
      "v_mfma_f32_16x16x32_bf16 v[192:195], a[232:235], a[168:171], v[192:195]\n"
      "ds_read_b64_tr_b16 a[196:197], v249 offset:36864\n"
      "v_mfma_f32_16x16x32_bf16 v[192:195], a[236:239], a[180:183], v[192:195]\n"
      "global_load_lds_dwordx4 v234, s[14:15] offset:3072\n"
      "v_mfma_f32_16x16x32_bf16 v[196:199], a[252:255], a[180:183], v[196:199]\n"
      "ds_read_b64_tr_b16 a[198:199], v249 offset:38912\n"
      "ds_read_b64_tr_b16 a[200:201], v223 offset:32768\n"
      "v_mfma_f32_16x16x32_bf16 v[196:199], a[248:251], a[168:171], v[196:199]\n"
      "s_add_u32 s3, s69, s23\n"
      "ds_read_b64_tr_b16 a[202:203], v223 offset:34816\n"
      "v_mfma_f32_16x16x32_bf16 v[196:199], a[244:247], v[168:171], v[196:199]\n"
      "ds_read_b64_tr_b16 a[204:205], v223 offset:36864\n"
      "v_mfma_f32_16x16x32_bf16 v[196:199], a[240:243], v[144:147], v[196:199]\n"
      "v_cvt_pk_bf16_f32 v12, v192, v193\n"
      "v_cvt_pk_bf16_f32 v13, v194, v195\n"
      "v_mfma_f32_16x16x32_bf16 v[200:203], a[224:227], v[148:151], v[200:203]\n"
      "v_bitop3_b32 v247, v12, v13, s47 bitop3:0x96\n"
      "ds_read_b64_tr_b16 a[206:207], v223 offset:38912\n"
      "v_mfma_f32_16x16x32_bf16 v[200:203], a[228:231], v[172:175], v[200:203]\n"
      "s_add_u32 s14, s12, s3\n"
      "s_addc_u32 s15, s13, 0\n"
      "v_mfma_f32_16x16x32_bf16 v[200:203], a[232:235], a[172:175], v[200:203]\n"
      "ds_read_b64_tr_b16 a[208:209], v250 offset:32768\n"
      "ds_read_b64_tr_b16 a[210:211], v250 offset:34816\n"
      "v_mfma_f32_16x16x32_bf16 v[200:203], a[236:239], a[184:187], v[200:203]\n"
      "v_cvt_pk_bf16_f32 v14, v196, v197\n"
      "v_cvt_pk_bf16_f32 v15, v198, v199\n"
      "v_mfma_f32_16x16x32_bf16 v[204:207], a[252:255], a[184:187], v[204:207]\n"
      "v_bitop3_b32 v247, v14, v15, v247 bitop3:0x96\n"
      "s_add_u32 m0, s28, 114688\n"
      "v_mfma_f32_16x16x32_bf16 v[204:207], a[248:251], a[172:175], v[204:207]\n"
      "ds_read_b64_tr_b16 a[212:213], v250 offset:36864\n"
      "ds_read_b64_tr_b16 a[214:215], v250 offset:38912\n"
      "v_mfma_f32_16x16x32_bf16 v[204:207], a[244:247], v[172:175], v[204:207]\n"
      "global_load_lds_dwordx4 v231, s[14:15] offset:0\n"
      "v_mfma_f32_16x16x32_bf16 v[204:207], a[240:243], v[148:151], v[204:207]\n"
      "v_cvt_pk_bf16_f32 v16, v200, v201\n"
      "v_cvt_pk_bf16_f32 v17, v202, v203\n"
      "v_mfma_f32_16x16x32_bf16 v[208:211], a[224:227], v[152:155], v[208:211]\n"
      "v_bitop3_b32 v247, v16, v17, v247 bitop3:0x96\n"
      "ds_read_b64_tr_b16 a[216:217], v224 offset:32768\n"
      "v_mfma_f32_16x16x32_bf16 v[208:211], a[228:231], v[176:179], v[208:211]\n"
      "ds_read_b64_tr_b16 a[218:219], v224 offset:34816\n"
      "v_mfma_f32_16x16x32_bf16 v[208:211], a[232:235], a[176:179], v[208:211]\n"
      "ds_read_b64_tr_b16 a[220:221], v224 offset:36864\n"
      "ds_read_b64_tr_b16 a[222:223], v224 offset:38912\n"
      "v_mfma_f32_16x16x32_bf16 v[208:211], a[236:239], a[188:191], v[208:211]\n"
      "v_cvt_pk_bf16_f32 v18, v204, v205\n"
      "v_cvt_pk_bf16_f32 v19, v206, v207\n"
      "v_mfma_f32_16x16x32_bf16 v[212:215], a[252:255], a[188:191], v[212:215]\n"
      "v_bitop3_b32 v247, v18, v19, v247 bitop3:0x96\n"
      "v_mfma_f32_16x16x32_bf16 v[212:215], a[248:251], a[176:179], v[212:215]\n"
      "global_load_lds_dwordx4 v232, s[14:15] offset:1024\n"
      "v_mfma_f32_16x16x32_bf16 v[212:215], a[244:247], v[176:179], v[212:215]\n"
      "s_waitcnt vmcnt(19) lgkmcnt(0)\n"
      "v_mfma_f32_16x16x32_bf16 v[212:215], a[240:243], v[152:155], v[212:215]\n"
      "s_barrier\n"
      "v_mfma_f32_16x16x32_bf16 v[192:195], a[192:195], v[0:3], 0\n"
      "ds_read_b64_tr_b16 a[224:225], v216 offset:0\n"
      "ds_read_b64_tr_b16 a[226:227], v216 offset:2048\n"
      "v_mfma_f32_16x16x32_bf16 v[192:195], a[196:199], v[24:27], v[192:195]\n"
      "v_cvt_pk_bf16_f32 v20, v208, v209\n"
      "v_cvt_pk_bf16_f32 v21, v210, v211\n"
      "v_mfma_f32_16x16x32_bf16 v[192:195], a[200:203], a[96:99], v[192:195]\n"
      "v_bitop3_b32 v247, v20, v21, v247 bitop3:0x96\n"
      "v_cvt_pk_bf16_f32 v22, v212, v213\n"
      "v_mfma_f32_16x16x32_bf16 v[192:195], a[204:207], a[108:111], v[192:195]\n"
      "v_cvt_pk_bf16_f32 v23, v214, v215\n"
      "v_bitop3_b32 v247, v22, v23, v247 bitop3:0x96\n"
      "ds_read_b64_tr_b16 a[228:229], v216 offset:4096\n"
      "v_mfma_f32_16x16x32_bf16 v[200:203], a[204:207], a[112:115], 0\n"
      "buffer_store_dwordx4 v[12:15], v253, s[24:27], s68 offen offset:0 sc1\n"
      "v_mfma_f32_16x16x32_bf16 v[200:203], a[200:203], a[100:103], v[200:203]\n"
      "ds_read_b64_tr_b16 a[230:231], v216 offset:6144\n"
      "ds_read_b64_tr_b16 a[232:233], v218 offset:0\n"
      "v_mfma_f32_16x16x32_bf16 v[200:203], a[196:199], v[28:31], v[200:203]\n"
      "buffer_store_dwordx4 v[16:19], v253, s[24:27], s68 offen offset:1024 sc1\n"
      "v_mfma_f32_16x16x32_bf16 v[200:203], a[192:195], v[4:7], v[200:203]\n"
      "ds_read_b64_tr_b16 a[234:235], v218 offset:2048\n"
      "ds_read_b64_tr_b16 a[236:237], v218 offset:4096\n"
      "v_mfma_f32_16x16x32_bf16 v[208:211], a[192:195], v[8:11], 0\n"
      "buffer_store_dwordx4 v[20:23], v253, s[24:27], s68 offen offset:2048 sc1\n"
      "v_mfma_f32_16x16x32_bf16 v[208:211], a[196:199], v[32:35], v[208:211]\n"
      "global_load_lds_dwordx4 v233, s[14:15] offset:2048\n"
      "v_mfma_f32_16x16x32_bf16 v[208:211], a[200:203], a[104:107], v[208:211]\n"
      "buffer_store_dword v247, v221, s[24:27], s68 offen offset:3072 sc1\n"
      "v_mfma_f32_16x16x32_bf16 v[208:211], a[204:207], a[116:119], v[208:211]\n"
      "ds_read_b64_tr_b16 a[238:239], v218 offset:6144\n"
      "ds_read_b64_tr_b16 a[240:241], v217 offset:0\n"
      "v_mfma_f32_16x16x32_bf16 v[196:199], a[208:211], v[0:3], 0\n"
      "v_mov_b32 v252, s37\n"
      "ds_read_b64_tr_b16 a[242:243], v217 offset:2048\n"
      "v_mfma_f32_16x16x32_bf16 v[196:199], a[212:215], v[24:27], v[196:199]\n"
      "buffer_store_dword v252, off, s[24:27], s36 offset:0 sc1\n"
      "v_mfma_f32_16x16x32_bf16 v[196:199], a[216:219], a[96:99], v[196:199]\n"
      "ds_read_b64_tr_b16 a[244:245], v217 offset:4096\n"
      "ds_read_b64_tr_b16 a[246:247], v217 offset:6144\n"
      "v_mfma_f32_16x16x32_bf16 v[196:199], a[220:223], a[108:111], v[196:199]\n"
      "s_add_u32 s37, s37, 1\n"
      "ds_read_b64_tr_b16 a[248:249], v219 offset:0\n"
      "v_mfma_f32_16x16x32_bf16 v[204:207], a[220:223], a[112:115], 0\n"
      "ds_read_b64_tr_b16 a[250:251], v219 offset:2048\n"
      "ds_read_b64_tr_b16 a[252:253], v219 offset:4096\n"
      "v_mfma_f32_16x16x32_bf16 v[204:207], a[216:219], a[100:103], v[204:207]\n"
      "s_add_u32 s68, s68, 13312\n"
      "ds_read_b64_tr_b16 a[254:255], v219 offset:6144\n"
      "v_mfma_f32_16x16x32_bf16 v[204:207], a[212:215], v[28:31], v[204:207]\n"
      "buffer_load_dwordx4 v[36:39], v253, s[24:27], s52 offen offset:0 sc1\n"
      "v_mfma_f32_16x16x32_bf16 v[204:207], a[208:211], v[4:7], v[204:207]\n"
      "v_mfma_f32_16x16x32_bf16 v[212:215], a[208:211], v[8:11], 0\n"
      "buffer_load_dwordx4 v[40:43], v253, s[24:27], s52 offen offset:1024 sc1\n"
      "v_mfma_f32_16x16x32_bf16 v[212:215], a[212:215], v[32:35], v[212:215]\n"
      "buffer_load_dwordx4 v[44:47], v253, s[24:27], s52 offen offset:2048 sc1\n"
      "buffer_load_dword v225, v221, s[24:27], s52 offen offset:3072 sc1\n"
      "v_mfma_f32_16x16x32_bf16 v[212:215], a[216:219], a[104:107], v[212:215]\n"
      "s_sub_u32 s49, s68, 93184\n"
      "s_waitcnt vmcnt(20) lgkmcnt(0)\n"
      "v_mfma_f32_16x16x32_bf16 v[212:215], a[220:223], a[116:119], v[212:215]\n"
      "s_cmp_eq_u32 s68, s79\n"
      "s_cselect_b32 s68, s49, s68\n"
      "s_barrier\n"
      "v_mfma_f32_16x16x32_bf16 v[192:195], a[224:227], v[48:51], v[192:195]\n"
      "ds_read_b64_tr_b16 a[192:193], v216 offset:16384\n"
      "v_mfma_f32_16x16x32_bf16 v[192:195], a[228:231], v[72:75], v[192:195]\n"
      "ds_read_b64_tr_b16 a[194:195], v216 offset:18432\n"
      "v_mfma_f32_16x16x32_bf16 v[192:195], a[232:235], a[120:123], v[192:195]\n"
      "global_load_lds_dwordx4 v234, s[14:15] offset:3072\n"
      "v_mfma_f32_16x16x32_bf16 v[192:195], a[236:239], a[132:135], v[192:195]\n"
      "ds_read_b64_tr_b16 a[196:197], v216 offset:20480\n"
      "v_mfma_f32_16x16x32_bf16 v[200:203], a[236:239], a[136:139], v[200:203]\n"
      "ds_read_b64_tr_b16 a[198:199], v216 offset:22528\n"
      "v_mfma_f32_16x16x32_bf16 v[200:203], a[232:235], a[124:127], v[200:203]\n"
      "ds_read_b64_tr_b16 a[200:201], v218 offset:16384\n"
      "v_mfma_f32_16x16x32_bf16 v[200:203], a[228:231], v[76:79], v[200:203]\n"
      "s_add_u32 s3, s69, s34\n"
      "ds_read_b64_tr_b16 a[202:203], v218 offset:18432\n"
      "v_mfma_f32_16x16x32_bf16 v[200:203], a[224:227], v[52:55], v[200:203]\n"
      "ds_read_b64_tr_b16 a[204:205], v218 offset:20480\n"
      "v_mfma_f32_16x16x32_bf16 v[208:211], a[224:227], v[56:59], v[208:211]\n"
      "ds_read_b64_tr_b16 a[206:207], v218 offset:22528\n"
      "v_mfma_f32_16x16x32_bf16 v[208:211], a[228:231], v[80:83], v[208:211]\n"
      "v_mfma_f32_16x16x32_bf16 v[208:211], a[232:235], a[128:131], v[208:211]\n"
      "s_add_u32 s14, s12, s3\n"
      "s_addc_u32 s15, s13, 0\n"
      "v_mfma_f32_16x16x32_bf16 v[208:211], a[236:239], a[140:143], v[208:211]\n"
      "ds_read_b64_tr_b16 a[208:209], v217 offset:16384\n"
      "v_mfma_f32_16x16x32_bf16 v[196:199], a[240:243], v[48:51], v[196:199]\n"
      "ds_read_b64_tr_b16 a[210:211], v217 offset:18432\n"
      "v_mfma_f32_16x16x32_bf16 v[196:199], a[244:247], v[72:75], v[196:199]\n"
      "ds_read_b64_tr_b16 a[212:213], v217 offset:20480\n"
      "v_mfma_f32_16x16x32_bf16 v[196:199], a[248:251], a[120:123], v[196:199]\n"
      "ds_read_b64_tr_b16 a[214:215], v217 offset:22528\n"
      "v_mfma_f32_16x16x32_bf16 v[196:199], a[252:255], a[132:135], v[196:199]\n"
      "s_add_u32 m0, s28, 131072\n"
      "ds_read_b64_tr_b16 a[216:217], v219 offset:16384\n"
      "v_mfma_f32_16x16x32_bf16 v[204:207], a[252:255], a[136:139], v[204:207]\n"
      "ds_read_b64_tr_b16 a[218:219], v219 offset:18432\n"
      "v_mfma_f32_16x16x32_bf16 v[204:207], a[248:251], a[124:127], v[204:207]\n"
      "ds_read_b64_tr_b16 a[220:221], v219 offset:20480\n"
      "v_mfma_f32_16x16x32_bf16 v[204:207], a[244:247], v[76:79], v[204:207]\n"
      "ds_read_b64_tr_b16 a[222:223], v219 offset:22528\n"
      "v_mfma_f32_16x16x32_bf16 v[204:207], a[240:243], v[52:55], v[204:207]\n"
      "global_load_lds_dwordx4 v231, s[14:15] offset:0\n"
      "v_mfma_f32_16x16x32_bf16 v[212:215], a[240:243], v[56:59], v[212:215]\n"
      "v_mfma_f32_16x16x32_bf16 v[212:215], a[244:247], v[80:83], v[212:215]\n"
      "v_mfma_f32_16x16x32_bf16 v[212:215], a[248:251], a[128:131], v[212:215]\n"
      "s_waitcnt vmcnt(18) lgkmcnt(0)\n"
      "v_mfma_f32_16x16x32_bf16 v[212:215], a[252:255], a[140:143], v[212:215]\n"
      "s_barrier\n"
      "v_mfma_f32_16x16x32_bf16 v[192:195], a[192:195], v[96:99], v[192:195]\n"
      "ds_read_b64_tr_b16 a[224:225], v216 offset:32768\n"
      "v_mfma_f32_16x16x32_bf16 v[192:195], a[196:199], v[120:123], v[192:195]\n"
      "global_load_lds_dwordx4 v232, s[14:15] offset:1024\n"
      "v_mfma_f32_16x16x32_bf16 v[192:195], a[200:203], a[144:147], v[192:195]\n"
      "ds_read_b64_tr_b16 a[226:227], v216 offset:34816\n"
      "v_mfma_f32_16x16x32_bf16 v[192:195], a[204:207], a[156:159], v[192:195]\n"
      "ds_read_b64_tr_b16 a[228:229], v216 offset:36864\n"
      "v_mfma_f32_16x16x32_bf16 v[200:203], a[204:207], a[160:163], v[200:203]\n"
      "global_load_lds_dwordx4 v233, s[14:15] offset:2048\n"
      "v_mfma_f32_16x16x32_bf16 v[200:203], a[200:203], a[148:151], v[200:203]\n"
      "ds_read_b64_tr_b16 a[230:231], v216 offset:38912\n"
      "ds_read_b64_tr_b16 a[232:233], v218 offset:32768\n"
      "v_mfma_f32_16x16x32_bf16 v[200:203], a[196:199], v[124:127], v[200:203]\n"
      "ds_read_b64_tr_b16 a[234:235], v218 offset:34816\n"
      "v_mfma_f32_16x16x32_bf16 v[200:203], a[192:195], v[100:103], v[200:203]\n"
      "global_load_lds_dwordx4 v234, s[14:15] offset:3072\n"
      "v_mfma_f32_16x16x32_bf16 v[208:211], a[192:195], v[104:107], v[208:211]\n"
      "ds_read_b64_tr_b16 a[236:237], v218 offset:36864\n"
      "v_mfma_f32_16x16x32_bf16 v[208:211], a[196:199], v[128:131], v[208:211]\n"
      "ds_read_b64_tr_b16 a[238:239], v218 offset:38912\n"
      "v_mfma_f32_16x16x32_bf16 v[208:211], a[200:203], a[152:155], v[208:211]\n"
      "s_add_u32 s3, s69, s35\n"
      "ds_read_b64_tr_b16 a[240:241], v217 offset:32768\n"
      "v_mfma_f32_16x16x32_bf16 v[208:211], a[204:207], a[164:167], v[208:211]\n"
      "ds_read_b64_tr_b16 a[242:243], v217 offset:34816\n"
      "v_mfma_f32_16x16x32_bf16 v[196:199], a[208:211], v[96:99], v[196:199]\n"
      "s_add_u32 s14, s12, s3\n"
      "s_addc_u32 s15, s13, 0\n"
      "v_mfma_f32_16x16x32_bf16 v[196:199], a[212:215], v[120:123], v[196:199]\n"
      "ds_read_b64_tr_b16 a[244:245], v217 offset:36864\n"
      "v_mfma_f32_16x16x32_bf16 v[196:199], a[216:219], a[144:147], v[196:199]\n"
      "s_add_u32 m0, s28, 147456\n"
      "ds_read_b64_tr_b16 a[246:247], v217 offset:38912\n"
      "v_mfma_f32_16x16x32_bf16 v[196:199], a[220:223], a[156:159], v[196:199]\n"
      "ds_read_b64_tr_b16 a[248:249], v219 offset:32768\n"
      "ds_read_b64_tr_b16 a[250:251], v219 offset:34816\n"
      "v_mfma_f32_16x16x32_bf16 v[204:207], a[220:223], a[160:163], v[204:207]\n"
      "ds_read_b64_tr_b16 a[252:253], v219 offset:36864\n"
      "v_mfma_f32_16x16x32_bf16 v[204:207], a[216:219], a[148:151], v[204:207]\n"
      "global_load_lds_dwordx4 v231, s[14:15] offset:0\n"
      "v_mfma_f32_16x16x32_bf16 v[204:207], a[212:215], v[124:127], v[204:207]\n"
      "ds_read_b64_tr_b16 a[254:255], v219 offset:38912\n"
      "v_mfma_f32_16x16x32_bf16 v[204:207], a[208:211], v[100:103], v[204:207]\n"
      "v_mfma_f32_16x16x32_bf16 v[212:215], a[208:211], v[104:107], v[212:215]\n"
      "global_load_lds_dwordx4 v232, s[14:15] offset:1024\n"
      "v_mfma_f32_16x16x32_bf16 v[212:215], a[212:215], v[128:131], v[212:215]\n"
      "v_mfma_f32_16x16x32_bf16 v[212:215], a[216:219], a[152:155], v[212:215]\n"
      "s_waitcnt vmcnt(19) lgkmcnt(0)\n"
      "v_mfma_f32_16x16x32_bf16 v[212:215], a[220:223], a[164:167], v[212:215]\n"
      "s_barrier\n"
      "v_mfma_f32_16x16x32_bf16 v[192:195], a[224:227], v[144:147], v[192:195]\n"
      "ds_read_b64_tr_b16 a[192:193], v249 offset:49152\n"
      "ds_read_b64_tr_b16 a[194:195], v249 offset:51200\n"
      "v_mfma_f32_16x16x32_bf16 v[192:195], a[228:231], v[168:171], v[192:195]\n"
      "global_load_lds_dwordx4 v233, s[14:15] offset:2048\n"
      "v_mfma_f32_16x16x32_bf16 v[192:195], a[232:235], a[168:171], v[192:195]\n"
      "ds_read_b64_tr_b16 a[196:197], v249 offset:53248\n"
      "ds_read_b64_tr_b16 a[198:199], v249 offset:55296\n"
      "v_mfma_f32_16x16x32_bf16 v[192:195], a[236:239], a[180:183], v[192:195]\n"
      "global_load_lds_dwordx4 v234, s[14:15] offset:3072\n"
      "v_mfma_f32_16x16x32_bf16 v[196:199], a[252:255], a[180:183], v[196:199]\n"
      "s_add_u32 s69, s69, 128\n"
      "ds_read_b64_tr_b16 a[200:201], v223 offset:49152\n"
      "v_mfma_f32_16x16x32_bf16 v[196:199], a[248:251], a[168:171], v[196:199]\n"
      "ds_read_b64_tr_b16 a[202:203], v223 offset:51200\n"
      "ds_read_b64_tr_b16 a[204:205], v223 offset:53248\n"
      "v_mfma_f32_16x16x32_bf16 v[196:199], a[244:247], v[168:171], v[196:199]\n"
      "s_cmp_eq_u32 s69, 896\n"
      "s_cselect_b32 s69, 0, s69\n"
      "v_mfma_f32_16x16x32_bf16 v[196:199], a[240:243], v[144:147], v[196:199]\n"
      "v_cvt_pk_bf16_f32 v12, v192, v193\n"
      "v_cvt_pk_bf16_f32 v13, v194, v195\n"
      "v_mfma_f32_16x16x32_bf16 v[200:203], a[224:227], v[148:151], v[200:203]\n"
      "v_bitop3_b32 v247, v12, v13, s47 bitop3:0x96\n"
      "ds_read_b64_tr_b16 a[206:207], v223 offset:55296\n"
      "v_mfma_f32_16x16x32_bf16 v[200:203], a[228:231], v[172:175], v[200:203]\n"
      "s_add_u32 s3, s69, s22\n"
      "ds_read_b64_tr_b16 a[208:209], v250 offset:49152\n"
      "v_mfma_f32_16x16x32_bf16 v[200:203], a[232:235], a[172:175], v[200:203]\n"
      "ds_read_b64_tr_b16 a[210:211], v250 offset:51200\n"
      "ds_read_b64_tr_b16 a[212:213], v250 offset:53248\n"
      "v_mfma_f32_16x16x32_bf16 v[200:203], a[236:239], a[184:187], v[200:203]\n"
      "v_cvt_pk_bf16_f32 v14, v196, v197\n"
      "v_cvt_pk_bf16_f32 v15, v198, v199\n"
      "v_mfma_f32_16x16x32_bf16 v[204:207], a[252:255], a[184:187], v[204:207]\n"
      "v_bitop3_b32 v247, v14, v15, v247 bitop3:0x96\n"
      "ds_read_b64_tr_b16 a[214:215], v250 offset:55296\n"
      "v_mfma_f32_16x16x32_bf16 v[204:207], a[248:251], a[172:175], v[204:207]\n"
      "s_add_u32 s14, s12, s3\n"
      "s_addc_u32 s15, s13, 0\n"
      "v_mfma_f32_16x16x32_bf16 v[204:207], a[244:247], v[172:175], v[204:207]\n"
      "s_add_u32 m0, s28, 0\n"
      "ds_read_b64_tr_b16 a[216:217], v224 offset:49152\n"
      "v_mfma_f32_16x16x32_bf16 v[204:207], a[240:243], v[148:151], v[204:207]\n"
      "v_cvt_pk_bf16_f32 v16, v200, v201\n"
      "v_cvt_pk_bf16_f32 v17, v202, v203\n"
      "v_mfma_f32_16x16x32_bf16 v[208:211], a[224:227], v[152:155], v[208:211]\n"
      "v_bitop3_b32 v247, v16, v17, v247 bitop3:0x96\n"
      "ds_read_b64_tr_b16 a[218:219], v224 offset:51200\n"
      "v_mfma_f32_16x16x32_bf16 v[208:211], a[228:231], v[176:179], v[208:211]\n"
      "global_load_lds_dwordx4 v231, s[14:15] offset:0\n"
      "v_mfma_f32_16x16x32_bf16 v[208:211], a[232:235], a[176:179], v[208:211]\n"
      "ds_read_b64_tr_b16 a[220:221], v224 offset:53248\n"
      "ds_read_b64_tr_b16 a[222:223], v224 offset:55296\n"
      "v_mfma_f32_16x16x32_bf16 v[208:211], a[236:239], a[188:191], v[208:211]\n"
      "v_cvt_pk_bf16_f32 v18, v204, v205\n"
      "v_cvt_pk_bf16_f32 v19, v206, v207\n"
      "v_mfma_f32_16x16x32_bf16 v[212:215], a[252:255], a[188:191], v[212:215]\n"
      "v_bitop3_b32 v247, v18, v19, v247 bitop3:0x96\n"
      "v_mfma_f32_16x16x32_bf16 v[212:215], a[248:251], a[176:179], v[212:215]\n"
      "global_load_lds_dwordx4 v232, s[14:15] offset:1024\n"
      "v_mfma_f32_16x16x32_bf16 v[212:215], a[244:247], v[176:179], v[212:215]\n"
      "s_waitcnt vmcnt(10) lgkmcnt(0)\n"
      "v_mfma_f32_16x16x32_bf16 v[212:215], a[240:243], v[152:155], v[212:215]\n"
      "s_barrier\n"
      "v_mfma_f32_16x16x32_bf16 v[192:195], a[192:195], v[0:3], 0\n"
      "ds_read_b64_tr_b16 a[224:225], v216 offset:49152\n"
      "ds_read_b64_tr_b16 a[226:227], v216 offset:51200\n"
      "v_mfma_f32_16x16x32_bf16 v[192:195], a[196:199], v[24:27], v[192:195]\n"
      "v_cvt_pk_bf16_f32 v20, v208, v209\n"
      "v_cvt_pk_bf16_f32 v21, v210, v211\n"
      "v_mfma_f32_16x16x32_bf16 v[192:195], a[200:203], a[96:99], v[192:195]\n"
      "v_bitop3_b32 v247, v20, v21, v247 bitop3:0x96\n"
      "v_cvt_pk_bf16_f32 v22, v212, v213\n"
      "v_mfma_f32_16x16x32_bf16 v[192:195], a[204:207], a[108:111], v[192:195]\n"
      "v_cvt_pk_bf16_f32 v23, v214, v215\n"
      "v_bitop3_b32 v247, v22, v23, v247 bitop3:0x96\n"
      "ds_read_b64_tr_b16 a[228:229], v216 offset:53248\n"
      "v_mfma_f32_16x16x32_bf16 v[200:203], a[204:207], a[112:115], 0\n"
      "buffer_store_dwordx4 v[12:15], v253, s[24:27], s68 offen offset:0 sc1\n"
      "v_mfma_f32_16x16x32_bf16 v[200:203], a[200:203], a[100:103], v[200:203]\n"
      "ds_read_b64_tr_b16 a[230:231], v216 offset:55296\n"
      "ds_read_b64_tr_b16 a[232:233], v218 offset:49152\n"
      "v_mfma_f32_16x16x32_bf16 v[200:203], a[196:199], v[28:31], v[200:203]\n"
      "buffer_store_dwordx4 v[16:19], v253, s[24:27], s68 offen offset:1024 sc1\n"
      "v_mfma_f32_16x16x32_bf16 v[200:203], a[192:195], v[4:7], v[200:203]\n"
      "ds_read_b64_tr_b16 a[234:235], v218 offset:51200\n"
      "ds_read_b64_tr_b16 a[236:237], v218 offset:53248\n"
      "v_mfma_f32_16x16x32_bf16 v[208:211], a[192:195], v[8:11], 0\n"
      "buffer_store_dwordx4 v[20:23], v253, s[24:27], s68 offen offset:2048 sc1\n"
      "v_mfma_f32_16x16x32_bf16 v[208:211], a[196:199], v[32:35], v[208:211]\n"
      "global_load_lds_dwordx4 v233, s[14:15] offset:2048\n"
      "v_mfma_f32_16x16x32_bf16 v[208:211], a[200:203], a[104:107], v[208:211]\n"
      "buffer_store_dword v247, v221, s[24:27], s68 offen offset:3072 sc1\n"
      "v_mfma_f32_16x16x32_bf16 v[208:211], a[204:207], a[116:119], v[208:211]\n"
      "ds_read_b64_tr_b16 a[238:239], v218 offset:55296\n"
      "ds_read_b64_tr_b16 a[240:241], v217 offset:49152\n"
      "v_mfma_f32_16x16x32_bf16 v[196:199], a[208:211], v[0:3], 0\n"
      "v_mov_b32 v252, s37\n"
      "ds_read_b64_tr_b16 a[242:243], v217 offset:51200\n"
      "v_mfma_f32_16x16x32_bf16 v[196:199], a[212:215], v[24:27], v[196:199]\n"
      "buffer_store_dword v252, off, s[24:27], s36 offset:0 sc1\n"
      "v_mfma_f32_16x16x32_bf16 v[196:199], a[216:219], a[96:99], v[196:199]\n"
      "ds_read_b64_tr_b16 a[244:245], v217 offset:53248\n"
      "ds_read_b64_tr_b16 a[246:247], v217 offset:55296\n"
      "v_mfma_f32_16x16x32_bf16 v[196:199], a[220:223], a[108:111], v[196:199]\n"
      "s_add_u32 s37, s37, 1\n"
      "ds_read_b64_tr_b16 a[248:249], v219 offset:49152\n"
      "v_mfma_f32_16x16x32_bf16 v[204:207], a[220:223], a[112:115], 0\n"
      "ds_read_b64_tr_b16 a[250:251], v219 offset:51200\n"
      "ds_read_b64_tr_b16 a[252:253], v219 offset:53248\n"
      "v_mfma_f32_16x16x32_bf16 v[204:207], a[216:219], a[100:103], v[204:207]\n"
      "s_add_u32 s68, s68, 13312\n"
      "ds_read_b64_tr_b16 a[254:255], v219 offset:55296\n"
      "v_mfma_f32_16x16x32_bf16 v[204:207], a[212:215], v[28:31], v[204:207]\n"
      "buffer_load_dwordx4 v[60:63], v253, s[24:27], s53 offen offset:0 sc1\n"
      "v_mfma_f32_16x16x32_bf16 v[204:207], a[208:211], v[4:7], v[204:207]\n"
      "v_mfma_f32_16x16x32_bf16 v[212:215], a[208:211], v[8:11], 0\n"
      "buffer_load_dwordx4 v[64:67], v253, s[24:27], s53 offen offset:1024 sc1\n"
      "v_mfma_f32_16x16x32_bf16 v[212:215], a[212:215], v[32:35], v[212:215]\n"
      "buffer_load_dwordx4 v[68:71], v253, s[24:27], s53 offen offset:2048 sc1\n"
      "buffer_load_dword v226, v221, s[24:27], s53 offen offset:3072 sc1\n"
      "v_mfma_f32_16x16x32_bf16 v[212:215], a[216:219], a[104:107], v[212:215]\n"
      "s_sub_u32 s49, s68, 93184\n"
      "s_waitcnt vmcnt(16) lgkmcnt(0)\n"
      "v_mfma_f32_16x16x32_bf16 v[212:215], a[220:223], a[116:119], v[212:215]\n"
      "s_cmp_eq_u32 s68, s79\n"
      "s_cselect_b32 s68, s49, s68\n"
      "s_barrier\n"
      "v_mfma_f32_16x16x32_bf16 v[192:195], a[224:227], v[48:51], v[192:195]\n"
      "ds_read_b64_tr_b16 a[192:193], v235 offset:0\n"
      "v_mfma_f32_16x16x32_bf16 v[192:195], a[228:231], v[72:75], v[192:195]\n"
      "ds_read_b64_tr_b16 a[194:195], v235 offset:2048\n"
      "v_mfma_f32_16x16x32_bf16 v[192:195], a[232:235], a[120:123], v[192:195]\n"
      "global_load_lds_dwordx4 v234, s[14:15] offset:3072\n"
      "v_mfma_f32_16x16x32_bf16 v[192:195], a[236:239], a[132:135], v[192:195]\n"
      "ds_read_b64_tr_b16 a[196:197], v235 offset:4096\n"
      "v_mfma_f32_16x16x32_bf16 v[200:203], a[236:239], a[136:139], v[200:203]\n"
      "ds_read_b64_tr_b16 a[198:199], v235 offset:6144\n"
      "v_mfma_f32_16x16x32_bf16 v[200:203], a[232:235], a[124:127], v[200:203]\n"
      "ds_read_b64_tr_b16 a[200:201], v237 offset:0\n"
      "v_mfma_f32_16x16x32_bf16 v[200:203], a[228:231], v[76:79], v[200:203]\n"
      "s_add_u32 s3, s69, s23\n"
      "ds_read_b64_tr_b16 a[202:203], v237 offset:2048\n"
      "v_mfma_f32_16x16x32_bf16 v[200:203], a[224:227], v[52:55], v[200:203]\n"
      "ds_read_b64_tr_b16 a[204:205], v237 offset:4096\n"
      "v_mfma_f32_16x16x32_bf16 v[208:211], a[224:227], v[56:59], v[208:211]\n"
      "ds_read_b64_tr_b16 a[206:207], v237 offset:6144\n"
      "v_mfma_f32_16x16x32_bf16 v[208:211], a[228:231], v[80:83], v[208:211]\n"
      "v_mfma_f32_16x16x32_bf16 v[208:211], a[232:235], a[128:131], v[208:211]\n"
      "s_add_u32 s14, s12, s3\n"
      "s_addc_u32 s15, s13, 0\n"
      "v_mfma_f32_16x16x32_bf16 v[208:211], a[236:239], a[140:143], v[208:211]\n"
      "ds_read_b64_tr_b16 a[208:209], v236 offset:0\n"
      "v_mfma_f32_16x16x32_bf16 v[196:199], a[240:243], v[48:51], v[196:199]\n"
      "ds_read_b64_tr_b16 a[210:211], v236 offset:2048\n"
      "v_mfma_f32_16x16x32_bf16 v[196:199], a[244:247], v[72:75], v[196:199]\n"
      "ds_read_b64_tr_b16 a[212:213], v236 offset:4096\n"
      "v_mfma_f32_16x16x32_bf16 v[196:199], a[248:251], a[120:123], v[196:199]\n"
      "ds_read_b64_tr_b16 a[214:215], v236 offset:6144\n"
      "v_mfma_f32_16x16x32_bf16 v[196:199], a[252:255], a[132:135], v[196:199]\n"
      "s_add_u32 m0, s28, 16384\n"
      "ds_read_b64_tr_b16 a[216:217], v238 offset:0\n"
      "v_mfma_f32_16x16x32_bf16 v[204:207], a[252:255], a[136:139], v[204:207]\n"
      "ds_read_b64_tr_b16 a[218:219], v238 offset:2048\n"
      "v_mfma_f32_16x16x32_bf16 v[204:207], a[248:251], a[124:127], v[204:207]\n"
      "ds_read_b64_tr_b16 a[220:221], v238 offset:4096\n"
      "v_mfma_f32_16x16x32_bf16 v[204:207], a[244:247], v[76:79], v[204:207]\n"
      "ds_read_b64_tr_b16 a[222:223], v238 offset:6144\n"
      "v_mfma_f32_16x16x32_bf16 v[204:207], a[240:243], v[52:55], v[204:207]\n"
      "global_load_lds_dwordx4 v231, s[14:15] offset:0\n"
      "v_mfma_f32_16x16x32_bf16 v[212:215], a[240:243], v[56:59], v[212:215]\n"
      "v_mfma_f32_16x16x32_bf16 v[212:215], a[244:247], v[80:83], v[212:215]\n"
      "v_mfma_f32_16x16x32_bf16 v[212:215], a[248:251], a[128:131], v[212:215]\n"
      "s_waitcnt vmcnt(14) lgkmcnt(0)\n"
      "v_mfma_f32_16x16x32_bf16 v[212:215], a[252:255], a[140:143], v[212:215]\n"
      "s_barrier\n"
      "v_mfma_f32_16x16x32_bf16 v[192:195], a[192:195], v[96:99], v[192:195]\n"
      "ds_read_b64_tr_b16 a[224:225], v235 offset:16384\n"
      "v_mfma_f32_16x16x32_bf16 v[192:195], a[196:199], v[120:123], v[192:195]\n"
      "global_load_lds_dwordx4 v232, s[14:15] offset:1024\n"
      "v_mfma_f32_16x16x32_bf16 v[192:195], a[200:203], a[144:147], v[192:195]\n"
      "ds_read_b64_tr_b16 a[226:227], v235 offset:18432\n"
      "v_mfma_f32_16x16x32_bf16 v[192:195], a[204:207], a[156:159], v[192:195]\n"
      "ds_read_b64_tr_b16 a[228:229], v235 offset:20480\n"
      "v_mfma_f32_16x16x32_bf16 v[200:203], a[204:207], a[160:163], v[200:203]\n"
      "global_load_lds_dwordx4 v233, s[14:15] offset:2048\n"
      "v_mfma_f32_16x16x32_bf16 v[200:203], a[200:203], a[148:151], v[200:203]\n"
      "ds_read_b64_tr_b16 a[230:231], v235 offset:22528\n"
      "ds_read_b64_tr_b16 a[232:233], v237 offset:16384\n"
      "v_mfma_f32_16x16x32_bf16 v[200:203], a[196:199], v[124:127], v[200:203]\n"
      "ds_read_b64_tr_b16 a[234:235], v237 offset:18432\n"
      "v_mfma_f32_16x16x32_bf16 v[200:203], a[192:195], v[100:103], v[200:203]\n"
      "global_load_lds_dwordx4 v234, s[14:15] offset:3072\n"
      "v_mfma_f32_16x16x32_bf16 v[208:211], a[192:195], v[104:107], v[208:211]\n"
      "ds_read_b64_tr_b16 a[236:237], v237 offset:20480\n"
      "v_mfma_f32_16x16x32_bf16 v[208:211], a[196:199], v[128:131], v[208:211]\n"
      "ds_read_b64_tr_b16 a[238:239], v237 offset:22528\n"
      "v_mfma_f32_16x16x32_bf16 v[208:211], a[200:203], a[152:155], v[208:211]\n"
      "s_add_u32 s3, s69, s34\n"
      "ds_read_b64_tr_b16 a[240:241], v236 offset:16384\n"
      "v_mfma_f32_16x16x32_bf16 v[208:211], a[204:207], a[164:167], v[208:211]\n"
      "ds_read_b64_tr_b16 a[242:243], v236 offset:18432\n"
      "v_mfma_f32_16x16x32_bf16 v[196:199], a[208:211], v[96:99], v[196:199]\n"
      "s_add_u32 s14, s12, s3\n"
      "s_addc_u32 s15, s13, 0\n"
      "v_mfma_f32_16x16x32_bf16 v[196:199], a[212:215], v[120:123], v[196:199]\n"
      "ds_read_b64_tr_b16 a[244:245], v236 offset:20480\n"
      "v_mfma_f32_16x16x32_bf16 v[196:199], a[216:219], a[144:147], v[196:199]\n"
      "s_add_u32 m0, s28, 32768\n"
      "ds_read_b64_tr_b16 a[246:247], v236 offset:22528\n"
      "v_mfma_f32_16x16x32_bf16 v[196:199], a[220:223], a[156:159], v[196:199]\n"
      "ds_read_b64_tr_b16 a[248:249], v238 offset:16384\n"
      "ds_read_b64_tr_b16 a[250:251], v238 offset:18432\n"
      "v_mfma_f32_16x16x32_bf16 v[204:207], a[220:223], a[160:163], v[204:207]\n"
      "ds_read_b64_tr_b16 a[252:253], v238 offset:20480\n"
      "v_mfma_f32_16x16x32_bf16 v[204:207], a[216:219], a[148:151], v[204:207]\n"
      "global_load_lds_dwordx4 v231, s[14:15] offset:0\n"
      "v_mfma_f32_16x16x32_bf16 v[204:207], a[212:215], v[124:127], v[204:207]\n"
      "ds_read_b64_tr_b16 a[254:255], v238 offset:22528\n"
      "v_mfma_f32_16x16x32_bf16 v[204:207], a[208:211], v[100:103], v[204:207]\n"
      "v_mfma_f32_16x16x32_bf16 v[212:215], a[208:211], v[104:107], v[212:215]\n"
      "global_load_lds_dwordx4 v232, s[14:15] offset:1024\n"
      "v_mfma_f32_16x16x32_bf16 v[212:215], a[212:215], v[128:131], v[212:215]\n"
      "v_mfma_f32_16x16x32_bf16 v[212:215], a[216:219], a[152:155], v[212:215]\n"
      "s_waitcnt vmcnt(6) lgkmcnt(0)\n"
      "v_mfma_f32_16x16x32_bf16 v[212:215], a[220:223], a[164:167], v[212:215]\n"
      "s_barrier\n"
      "v_mfma_f32_16x16x32_bf16 v[192:195], a[224:227], v[144:147], v[192:195]\n"
      "buffer_load_dwordx4 v[84:87], v253, s[24:27], s54 offen offset:0 sc1\n"
      "v_mfma_f32_16x16x32_bf16 v[192:195], a[228:231], v[168:171], v[192:195]\n"
      "ds_read_b64_tr_b16 a[192:193], v249 offset:0\n"
      "ds_read_b64_tr_b16 a[194:195], v249 offset:2048\n"
      "v_mfma_f32_16x16x32_bf16 v[192:195], a[232:235], a[168:171], v[192:195]\n"
      "buffer_load_dwordx4 v[88:91], v253, s[24:27], s54 offen offset:1024 sc1\n"
      "v_mfma_f32_16x16x32_bf16 v[192:195], a[236:239], a[180:183], v[192:195]\n"
      "buffer_load_dwordx4 v[92:95], v253, s[24:27], s54 offen offset:2048 sc1\n"
      "v_mfma_f32_16x16x32_bf16 v[196:199], a[252:255], a[180:183], v[196:199]\n"
      "ds_read_b64_tr_b16 a[196:197], v249 offset:4096\n"
      "ds_read_b64_tr_b16 a[198:199], v249 offset:6144\n"
      "v_mfma_f32_16x16x32_bf16 v[196:199], a[248:251], a[168:171], v[196:199]\n"
      "buffer_load_dword v227, v221, s[24:27], s54 offen offset:3072 sc1\n"
      "v_mfma_f32_16x16x32_bf16 v[196:199], a[244:247], v[168:171], v[196:199]\n"
      "ds_read_b64_tr_b16 a[200:201], v223 offset:0\n"
      "ds_read_b64_tr_b16 a[202:203], v223 offset:2048\n"
      "v_mfma_f32_16x16x32_bf16 v[196:199], a[240:243], v[144:147], v[196:199]\n"
      "v_cvt_pk_bf16_f32 v12, v192, v193\n"
      "v_cvt_pk_bf16_f32 v13, v194, v195\n"
      "ds_read_b64_tr_b16 a[204:205], v223 offset:4096\n"
      "v_mfma_f32_16x16x32_bf16 v[200:203], a[224:227], v[148:151], v[200:203]\n"
      "v_bitop3_b32 v247, v12, v13, s47 bitop3:0x96\n"
      "ds_read_b64_tr_b16 a[206:207], v223 offset:6144\n"
      "ds_read_b64_tr_b16 a[208:209], v250 offset:0\n"
      "ds_read_b64_tr_b16 a[210:211], v250 offset:2048\n"
      "v_mfma_f32_16x16x32_bf16 v[200:203], a[228:231], v[172:175], v[200:203]\n"
      "global_load_lds_dwordx4 v233, s[14:15] offset:2048\n"
      "v_mfma_f32_16x16x32_bf16 v[200:203], a[232:235], a[172:175], v[200:203]\n"
      "global_load_lds_dwordx4 v234, s[14:15] offset:3072\n"
      "v_mfma_f32_16x16x32_bf16 v[200:203], a[236:239], a[184:187], v[200:203]\n"
      "s_add_u32 s3, s69, s35\n"
      "ds_read_b64_tr_b16 a[212:213], v250 offset:4096\n"
      "v_mfma_f32_16x16x32_bf16 v[204:207], a[252:255], a[184:187], v[204:207]\n"
      "v_cvt_pk_bf16_f32 v14, v196, v197\n"
      "ds_read_b64_tr_b16 a[214:215], v250 offset:6144\n"
      "v_mfma_f32_16x16x32_bf16 v[204:207], a[248:251], a[172:175], v[204:207]\n"
      "ds_read_b64_tr_b16 a[216:217], v224 offset:0\n"
      "v_mfma_f32_16x16x32_bf16 v[204:207], a[244:247], v[172:175], v[204:207]\n"
      "v_cvt_pk_bf16_f32 v15, v198, v199\n"
      "ds_read_b64_tr_b16 a[218:219], v224 offset:2048\n"
      "v_mfma_f32_16x16x32_bf16 v[204:207], a[240:243], v[148:151], v[204:207]\n"
      "s_add_u32 s14, s12, s3\n"
      "s_addc_u32 s15, s13, 0\n"
      "v_mfma_f32_16x16x32_bf16 v[208:211], a[224:227], v[152:155], v[208:211]\n"
      "s_add_u32 m0, s28, 65536\n"
      "ds_read_b64_tr_b16 a[220:221], v224 offset:4096\n"
      "v_mfma_f32_16x16x32_bf16 v[208:211], a[228:231], v[176:179], v[208:211]\n"
      "v_bitop3_b32 v247, v14, v15, v247 bitop3:0x96\n"
      "ds_read_b64_tr_b16 a[222:223], v224 offset:6144\n"
      "v_mfma_f32_16x16x32_bf16 v[208:211], a[232:235], a[176:179], v[208:211]\n"
      "global_load_lds_dwordx4 v231, s[14:15] offset:0\n"
      "v_mfma_f32_16x16x32_bf16 v[208:211], a[236:239], a[188:191], v[208:211]\n"
      "v_cvt_pk_bf16_f32 v16, v200, v201\n"
      "v_cvt_pk_bf16_f32 v17, v202, v203\n"
      "v_mfma_f32_16x16x32_bf16 v[212:215], a[252:255], a[188:191], v[212:215]\n"
      "v_bitop3_b32 v247, v16, v17, v247 bitop3:0x96\n"
      "v_cvt_pk_bf16_f32 v18, v204, v205\n"
      "v_mfma_f32_16x16x32_bf16 v[212:215], a[248:251], a[176:179], v[212:215]\n"
      "global_load_lds_dwordx4 v232, s[14:15] offset:1024\n"
      "v_mfma_f32_16x16x32_bf16 v[212:215], a[244:247], v[176:179], v[212:215]\n"
      "v_cvt_pk_bf16_f32 v19, v206, v207\n"
      "s_waitcnt vmcnt(10) lgkmcnt(0)\n"
      "v_mfma_f32_16x16x32_bf16 v[212:215], a[240:243], v[152:155], v[212:215]\n"
      "v_bitop3_b32 v247, v18, v19, v247 bitop3:0x96\n"
      "s_barrier\n"
      "v_mfma_f32_16x16x32_bf16 v[192:195], a[192:195], v[0:3], 0\n"
      "ds_read_b64_tr_b16 a[224:225], v249 offset:16384\n"
      "ds_read_b64_tr_b16 a[226:227], v249 offset:18432\n"
      "v_mfma_f32_16x16x32_bf16 v[192:195], a[196:199], v[24:27], v[192:195]\n"
      "v_cvt_pk_bf16_f32 v20, v208, v209\n"
      "v_cvt_pk_bf16_f32 v21, v210, v211\n"
      "ds_read_b64_tr_b16 a[228:229], v249 offset:20480\n"
      "v_mfma_f32_16x16x32_bf16 v[192:195], a[200:203], a[96:99], v[192:195]\n"
      "v_bitop3_b32 v247, v20, v21, v247 bitop3:0x96\n"
      "v_cvt_pk_bf16_f32 v22, v212, v213\n"
      "v_mfma_f32_16x16x32_bf16 v[192:195], a[204:207], a[108:111], v[192:195]\n"
      "v_cvt_pk_bf16_f32 v23, v214, v215\n"
      "v_bitop3_b32 v247, v22, v23, v247 bitop3:0x96\n"
      "v_mfma_f32_16x16x32_bf16 v[200:203], a[204:207], a[112:115], 0\n"
      "buffer_store_dwordx4 v[12:15], v253, s[24:27], s68 offen offset:0 sc1\n"
      "v_mfma_f32_16x16x32_bf16 v[200:203], a[200:203], a[100:103], v[200:203]\n"
      "ds_read_b64_tr_b16 a[230:231], v249 offset:22528\n"
      "ds_read_b64_tr_b16 a[232:233], v223 offset:16384\n"
      "v_mfma_f32_16x16x32_bf16 v[200:203], a[196:199], v[28:31], v[200:203]\n"
      "buffer_store_dwordx4 v[16:19], v253, s[24:27], s68 offen offset:1024 sc1\n"
      "v_mfma_f32_16x16x32_bf16 v[200:203], a[192:195], v[4:7], v[200:203]\n"
      "global_load_lds_dwordx4 v233, s[14:15] offset:2048\n"
      "v_mfma_f32_16x16x32_bf16 v[208:211], a[192:195], v[8:11], 0\n"
      "buffer_store_dwordx4 v[20:23], v253, s[24:27], s68 offen offset:2048 sc1\n"
      "v_mfma_f32_16x16x32_bf16 v[208:211], a[196:199], v[32:35], v[208:211]\n"
      "ds_read_b64_tr_b16 a[234:235], v223 offset:18432\n"
      "v_mfma_f32_16x16x32_bf16 v[208:211], a[200:203], a[104:107], v[208:211]\n"
      "buffer_store_dword v247, v221, s[24:27], s68 offen offset:3072 sc1\n"
      "v_mfma_f32_16x16x32_bf16 v[208:211], a[204:207], a[116:119], v[208:211]\n"
      "ds_read_b64_tr_b16 a[236:237], v223 offset:20480\n"
      "ds_read_b64_tr_b16 a[238:239], v223 offset:22528\n"
      "v_mfma_f32_16x16x32_bf16 v[196:199], a[208:211], v[0:3], 0\n"
      "v_mov_b32 v252, s37\n"
      "ds_read_b64_tr_b16 a[240:241], v250 offset:16384\n"
      "v_mfma_f32_16x16x32_bf16 v[196:199], a[212:215], v[24:27], v[196:199]\n"
      "buffer_store_dword v252, off, s[24:27], s36 offset:0 sc1\n"
      "v_mfma_f32_16x16x32_bf16 v[196:199], a[216:219], a[96:99], v[196:199]\n"
      "ds_read_b64_tr_b16 a[242:243], v250 offset:18432\n"
      "ds_read_b64_tr_b16 a[244:245], v250 offset:20480\n"
      "v_mfma_f32_16x16x32_bf16 v[196:199], a[220:223], a[108:111], v[196:199]\n"
      "s_add_u32 s37, s37, 1\n"
      "ds_read_b64_tr_b16 a[246:247], v250 offset:22528\n"
      "v_mfma_f32_16x16x32_bf16 v[204:207], a[220:223], a[112:115], 0\n"
      "ds_read_b64_tr_b16 a[248:249], v224 offset:16384\n"
      "v_mfma_f32_16x16x32_bf16 v[204:207], a[216:219], a[100:103], v[204:207]\n"
      "ds_read_b64_tr_b16 a[250:251], v224 offset:18432\n"
      "v_mfma_f32_16x16x32_bf16 v[204:207], a[212:215], v[28:31], v[204:207]\n"
      "ds_read_b64_tr_b16 a[252:253], v224 offset:20480\n"
      "ds_read_b64_tr_b16 a[254:255], v224 offset:22528\n"
      "v_mfma_f32_16x16x32_bf16 v[204:207], a[208:211], v[4:7], v[204:207]\n"
      "global_load_lds_dwordx4 v234, s[14:15] offset:3072\n"
      "v_mfma_f32_16x16x32_bf16 v[212:215], a[208:211], v[8:11], 0\n"
      "s_add_u32 s68, s68, 13312\n"
      "s_sub_u32 s49, s68, 93184\n"
      "v_mfma_f32_16x16x32_bf16 v[212:215], a[212:215], v[32:35], v[212:215]\n"
      "s_cmp_eq_u32 s68, s79\n"
      "s_cselect_b32 s68, s49, s68\n"
      "v_mfma_f32_16x16x32_bf16 v[212:215], a[216:219], a[104:107], v[212:215]\n"
      "s_waitcnt vmcnt(9) lgkmcnt(0)\n"
      "v_mfma_f32_16x16x32_bf16 v[212:215], a[220:223], a[116:119], v[212:215]\n"
      "s_barrier\n"
      "v_mfma_f32_16x16x32_bf16 v[192:195], a[224:227], v[48:51], v[192:195]\n"
      "ds_read_b64_tr_b16 a[192:193], v249 offset:32768\n"
      "v_mfma_f32_16x16x32_bf16 v[192:195], a[228:231], v[72:75], v[192:195]\n"
      "ds_read_b64_tr_b16 a[194:195], v249 offset:34816\n"
      "v_mfma_f32_16x16x32_bf16 v[192:195], a[232:235], a[120:123], v[192:195]\n"
      "buffer_load_dwordx4 v[108:111], v253, s[24:27], s55 offen offset:0 sc1\n"
      "v_mfma_f32_16x16x32_bf16 v[192:195], a[236:239], a[132:135], v[192:195]\n"
      "ds_read_b64_tr_b16 a[196:197], v249 offset:36864\n"
      "v_mfma_f32_16x16x32_bf16 v[200:203], a[236:239], a[136:139], v[200:203]\n"
      "ds_read_b64_tr_b16 a[198:199], v249 offset:38912\n"
      "v_mfma_f32_16x16x32_bf16 v[200:203], a[232:235], a[124:127], v[200:203]\n"
      "ds_read_b64_tr_b16 a[200:201], v223 offset:32768\n"
      "v_mfma_f32_16x16x32_bf16 v[200:203], a[228:231], v[76:79], v[200:203]\n"
      "ds_read_b64_tr_b16 a[202:203], v223 offset:34816\n"
      "v_mfma_f32_16x16x32_bf16 v[200:203], a[224:227], v[52:55], v[200:203]\n"
      "ds_read_b64_tr_b16 a[204:205], v223 offset:36864\n"
      "v_mfma_f32_16x16x32_bf16 v[208:211], a[224:227], v[56:59], v[208:211]\n"
      "buffer_load_dwordx4 v[112:115], v253, s[24:27], s55 offen offset:1024 sc1\n"
      "v_mfma_f32_16x16x32_bf16 v[208:211], a[228:231], v[80:83], v[208:211]\n"
      "ds_read_b64_tr_b16 a[206:207], v223 offset:38912\n"
      "v_mfma_f32_16x16x32_bf16 v[208:211], a[232:235], a[128:131], v[208:211]\n"
      "ds_read_b64_tr_b16 a[208:209], v250 offset:32768\n"
      "v_mfma_f32_16x16x32_bf16 v[208:211], a[236:239], a[140:143], v[208:211]\n"
      "ds_read_b64_tr_b16 a[210:211], v250 offset:34816\n"
      "v_mfma_f32_16x16x32_bf16 v[196:199], a[240:243], v[48:51], v[196:199]\n"
      "ds_read_b64_tr_b16 a[212:213], v250 offset:36864\n"
      "v_mfma_f32_16x16x32_bf16 v[196:199], a[244:247], v[72:75], v[196:199]\n"
      "buffer_load_dwordx4 v[116:119], v253, s[24:27], s55 offen offset:2048 sc1\n"
      "v_mfma_f32_16x16x32_bf16 v[196:199], a[248:251], a[120:123], v[196:199]\n"
      "ds_read_b64_tr_b16 a[214:215], v250 offset:38912\n"
      "v_mfma_f32_16x16x32_bf16 v[196:199], a[252:255], a[132:135], v[196:199]\n"
      "ds_read_b64_tr_b16 a[216:217], v224 offset:32768\n"
      "v_mfma_f32_16x16x32_bf16 v[204:207], a[252:255], a[136:139], v[204:207]\n"
      "ds_read_b64_tr_b16 a[218:219], v224 offset:34816\n"
      "v_mfma_f32_16x16x32_bf16 v[204:207], a[248:251], a[124:127], v[204:207]\n"
      "ds_read_b64_tr_b16 a[220:221], v224 offset:36864\n"
      "v_mfma_f32_16x16x32_bf16 v[204:207], a[244:247], v[76:79], v[204:207]\n"
      "ds_read_b64_tr_b16 a[222:223], v224 offset:38912\n"
      "v_mfma_f32_16x16x32_bf16 v[204:207], a[240:243], v[52:55], v[204:207]\n"
      "buffer_load_dword v228, v221, s[24:27], s55 offen offset:3072 sc1\n"
      "v_mfma_f32_16x16x32_bf16 v[212:215], a[240:243], v[56:59], v[212:215]\n"
      "v_mfma_f32_16x16x32_bf16 v[212:215], a[244:247], v[80:83], v[212:215]\n"
      "v_mfma_f32_16x16x32_bf16 v[212:215], a[248:251], a[128:131], v[212:215]\n"
      "s_waitcnt vmcnt(4) lgkmcnt(0)\n"
      "v_mfma_f32_16x16x32_bf16 v[212:215], a[252:255], a[140:143], v[212:215]\n"
      "s_barrier\n"
      "v_mfma_f32_16x16x32_bf16 v[192:195], a[192:195], v[96:99], v[192:195]\n"
      "ds_read_b64_tr_b16 a[224:225], v216 offset:0\n"
      "v_mfma_f32_16x16x32_bf16 v[192:195], a[196:199], v[120:123], v[192:195]\n"
      "ds_read_b64_tr_b16 a[226:227], v216 offset:2048\n"
      "v_mfma_f32_16x16x32_bf16 v[192:195], a[200:203], a[144:147], v[192:195]\n"
      "ds_read_b64_tr_b16 a[228:229], v216 offset:4096\n"
      "v_mfma_f32_16x16x32_bf16 v[192:195], a[204:207], a[156:159], v[192:195]\n"
      "v_mfma_f32_16x16x32_bf16 v[200:203], a[204:207], a[160:163], v[200:203]\n"
      "ds_read_b64_tr_b16 a[230:231], v216 offset:6144\n"
      "v_mfma_f32_16x16x32_bf16 v[200:203], a[200:203], a[148:151], v[200:203]\n"
      "ds_read_b64_tr_b16 a[232:233], v218 offset:0\n"
      "v_mfma_f32_16x16x32_bf16 v[200:203], a[196:199], v[124:127], v[200:203]\n"
      "ds_read_b64_tr_b16 a[234:235], v218 offset:2048\n"
      "v_mfma_f32_16x16x32_bf16 v[200:203], a[192:195], v[100:103], v[200:203]\n"
      "ds_read_b64_tr_b16 a[236:237], v218 offset:4096\n"
      "v_mfma_f32_16x16x32_bf16 v[208:211], a[192:195], v[104:107], v[208:211]\n"
      "ds_read_b64_tr_b16 a[238:239], v218 offset:6144\n"
      "v_mfma_f32_16x16x32_bf16 v[208:211], a[196:199], v[128:131], v[208:211]\n"
      "v_mfma_f32_16x16x32_bf16 v[208:211], a[200:203], a[152:155], v[208:211]\n"
      "ds_read_b64_tr_b16 a[240:241], v217 offset:0\n"
      "v_mfma_f32_16x16x32_bf16 v[208:211], a[204:207], a[164:167], v[208:211]\n"
      "ds_read_b64_tr_b16 a[242:243], v217 offset:2048\n"
      "v_mfma_f32_16x16x32_bf16 v[196:199], a[208:211], v[96:99], v[196:199]\n"
      "ds_read_b64_tr_b16 a[244:245], v217 offset:4096\n"
      "v_mfma_f32_16x16x32_bf16 v[196:199], a[212:215], v[120:123], v[196:199]\n"
      "ds_read_b64_tr_b16 a[246:247], v217 offset:6144\n"
      "v_mfma_f32_16x16x32_bf16 v[196:199], a[216:219], a[144:147], v[196:199]\n"
      "ds_read_b64_tr_b16 a[248:249], v219 offset:0\n"
      "v_mfma_f32_16x16x32_bf16 v[196:199], a[220:223], a[156:159], v[196:199]\n"
      "v_mfma_f32_16x16x32_bf16 v[204:207], a[220:223], a[160:163], v[204:207]\n"
      "ds_read_b64_tr_b16 a[250:251], v219 offset:2048\n"
      "v_mfma_f32_16x16x32_bf16 v[204:207], a[216:219], a[148:151], v[204:207]\n"
      "ds_read_b64_tr_b16 a[252:253], v219 offset:4096\n"
      "v_mfma_f32_16x16x32_bf16 v[204:207], a[212:215], v[124:127], v[204:207]\n"
      "ds_read_b64_tr_b16 a[254:255], v219 offset:6144\n"
      "v_mfma_f32_16x16x32_bf16 v[204:207], a[208:211], v[100:103], v[204:207]\n"
      "v_mfma_f32_16x16x32_bf16 v[212:215], a[208:211], v[104:107], v[212:215]\n"
      "v_mfma_f32_16x16x32_bf16 v[212:215], a[212:215], v[128:131], v[212:215]\n"
      "v_mfma_f32_16x16x32_bf16 v[212:215], a[216:219], a[152:155], v[212:215]\n"
      "s_waitcnt lgkmcnt(0)\n"
      "v_mfma_f32_16x16x32_bf16 v[212:215], a[220:223], a[164:167], v[212:215]\n"
      "v_mfma_f32_16x16x32_bf16 v[192:195], a[224:227], v[144:147], v[192:195]\n"
      "v_mfma_f32_16x16x32_bf16 v[192:195], a[228:231], v[168:171], v[192:195]\n"
      "v_mfma_f32_16x16x32_bf16 v[192:195], a[232:235], a[168:171], v[192:195]\n"
      "v_mfma_f32_16x16x32_bf16 v[192:195], a[236:239], a[180:183], v[192:195]\n"
      "v_mfma_f32_16x16x32_bf16 v[196:199], a[252:255], a[180:183], v[196:199]\n"
      "v_mfma_f32_16x16x32_bf16 v[196:199], a[248:251], a[168:171], v[196:199]\n"
      "v_mov_b32 v251, 0\n"
      "v_mfma_f32_16x16x32_bf16 v[196:199], a[244:247], v[168:171], v[196:199]\n"
      "s_mov_b64 exec, 0xff\n"
      "buffer_load_dword v251, v220, s[24:27], 0 offen sc1\n"
      "s_mov_b64 exec, -1\n"
      "v_mfma_f32_16x16x32_bf16 v[196:199], a[240:243], v[144:147], v[196:199]\n"
      "v_mfma_f32_16x16x32_bf16 v[200:203], a[224:227], v[148:151], v[200:203]\n"
      "v_mfma_f32_16x16x32_bf16 v[200:203], a[228:231], v[172:175], v[200:203]\n"
      "v_cvt_pk_bf16_f32 v12, v192, v193\n"
      "v_mfma_f32_16x16x32_bf16 v[200:203], a[232:235], a[172:175], v[200:203]\n"
      "v_cvt_pk_bf16_f32 v13, v194, v195\n"
      "v_mfma_f32_16x16x32_bf16 v[200:203], a[236:239], a[184:187], v[200:203]\n"
      "v_bitop3_b32 v247, v12, v13, s47 bitop3:0x96\n"
      "v_mfma_f32_16x16x32_bf16 v[204:207], a[252:255], a[184:187], v[204:207]\n"
      "v_mfma_f32_16x16x32_bf16 v[204:207], a[248:251], a[172:175], v[204:207]\n"
      "v_cvt_pk_bf16_f32 v14, v196, v197\n"
      "v_cvt_pk_bf16_f32 v15, v198, v199\n"
      "v_mfma_f32_16x16x32_bf16 v[204:207], a[244:247], v[172:175], v[204:207]\n"
      "v_bitop3_b32 v247, v14, v15, v247 bitop3:0x96\n"
      "v_mfma_f32_16x16x32_bf16 v[204:207], a[240:243], v[148:151], v[204:207]\n"
      "v_mfma_f32_16x16x32_bf16 v[208:211], a[224:227], v[152:155], v[208:211]\n"
      "v_mfma_f32_16x16x32_bf16 v[208:211], a[228:231], v[176:179], v[208:211]\n"
      "v_cvt_pk_bf16_f32 v16, v200, v201\n"
      "v_cvt_pk_bf16_f32 v17, v202, v203\n"
      "v_mfma_f32_16x16x32_bf16 v[208:211], a[232:235], a[176:179], v[208:211]\n"
      "v_bitop3_b32 v247, v16, v17, v247 bitop3:0x96\n"
      "v_mfma_f32_16x16x32_bf16 v[208:211], a[236:239], a[188:191], v[208:211]\n"
      "v_mfma_f32_16x16x32_bf16 v[212:215], a[252:255], a[188:191], v[212:215]\n"
      "v_mfma_f32_16x16x32_bf16 v[212:215], a[248:251], a[176:179], v[212:215]\n"
      "v_cvt_pk_bf16_f32 v18, v204, v205\n"
      "v_mfma_f32_16x16x32_bf16 v[212:215], a[244:247], v[176:179], v[212:215]\n"
      "v_cvt_pk_bf16_f32 v19, v206, v207\n"
      "v_bitop3_b32 v247, v18, v19, v247 bitop3:0x96\n"
      "v_mfma_f32_16x16x32_bf16 v[212:215], a[240:243], v[152:155], v[212:215]\n"
      "s_waitcnt vmcnt(0)\n"
      "v_readlane_b32 s21, v251, 4\n"
      "v_readlane_b32 s44, v251, 5\n"
      "v_readlane_b32 s45, v251, 6\n"
      "s_nop 1\n"
      "s_sub_u32 s21, s21, s19\n"
      "s_sub_u32 s44, s44, s38\n"
      "s_sub_u32 s45, s45, s39\n"
      "s_cmp_le_u32 s21, 6\n"
      "s_cselect_b32 s21, s56, 25559040\n"
      "s_cmp_le_u32 s44, 6\n"
      "s_cselect_b32 s44, s57, 25559040\n"
      "s_cmp_le_u32 s45, 6\n"
      "s_cselect_b32 s45, s58, 25559040\n"
      "buffer_load_dwordx4 v[132:135], v253, s[24:27], s21 offen offset:0 sc1\n"
      "buffer_load_dwordx4 v[136:139], v253, s[24:27], s21 offen offset:1024 sc1\n"
      "buffer_load_dwordx4 v[140:143], v253, s[24:27], s21 offen offset:2048 sc1\n"
      "buffer_load_dword v229, v221, s[24:27], s21 offen offset:3072 sc1\n"
      "buffer_load_dwordx4 v[156:159], v253, s[24:27], s44 offen offset:0 sc1\n"
      "buffer_load_dwordx4 v[160:163], v253, s[24:27], s44 offen offset:1024 sc1\n"
      "buffer_load_dwordx4 v[164:167], v253, s[24:27], s44 offen offset:2048 sc1\n"
      "buffer_load_dword v230, v221, s[24:27], s44 offen offset:3072 sc1\n"
      "buffer_load_dwordx4 v[180:183], v253, s[24:27], s45 offen offset:0 sc1\n"
      "buffer_load_dwordx4 v[184:187], v253, s[24:27], s45 offen offset:1024 sc1\n"
      "buffer_load_dwordx4 v[188:191], v253, s[24:27], s45 offen offset:2048 sc1\n"
      "buffer_load_dword v246, v221, s[24:27], s45 offen offset:3072 sc1\n"
      "v_sub_u32 v251, v251, v254\n"
      "v_cmp_lt_u32 vcc, 6, v251\n"
      "s_nop 3\n"
      "s_and_b32 s74, vcc_lo, 0xff\n"
      "s_cbranch_scc1 .Lfa_e\n"
      ".Lfa_e_ok:\n"
      "buffer_store_dword v255, off, s[24:27], s66 offset:0 sc1\n"
      "s_cmp_eq_u32 s46, 7\n"
      "s_cbranch_scc1 .Lfa_last\n"
      "v_bitop3_b32 v248, v36, v37, v225 bitop3:0x96\n"
      "v_bitop3_b32 v248, v38, v39, v248 bitop3:0x96\n"
      "v_bitop3_b32 v248, v40, v41, v248 bitop3:0x96\n"
      "v_bitop3_b32 v248, v42, v43, v248 bitop3:0x96\n"
      "v_bitop3_b32 v248, v44, v45, v248 bitop3:0x96\n"
      "v_bitop3_b32 v248, v46, v47, v248 bitop3:0x96\n"
      "v_cmp_ne_u32 vcc, s59, v248\n"
      "s_cbranch_vccnz .Lfa_m0\n"
      ".Lfa_m0_ok:\n"
      "v_mfma_f32_16x16x32_bf16 v[192:195], v[242:245], v[36:39], v[192:195]\n"
      "v_mfma_f32_16x16x32_bf16 v[200:203], v[242:245], v[40:43], v[200:203]\n"
      "v_mfma_f32_16x16x32_bf16 v[208:211], v[242:245], v[44:47], v[208:211]\n"
      "v_mfma_f32_16x16x32_bf16 v[196:199], v[240:243], v[36:39], v[196:199]\n"
      "v_mfma_f32_16x16x32_bf16 v[204:207], v[240:243], v[40:43], v[204:207]\n"
      "v_mfma_f32_16x16x32_bf16 v[212:215], v[240:243], v[44:47], v[212:215]\n"
      "v_bitop3_b32 v248, v60, v61, v226 bitop3:0x96\n"
      "v_bitop3_b32 v248, v62, v63, v248 bitop3:0x96\n"
      "v_bitop3_b32 v248, v64, v65, v248 bitop3:0x96\n"
      "v_bitop3_b32 v248, v66, v67, v248 bitop3:0x96\n"
      "v_bitop3_b32 v248, v68, v69, v248 bitop3:0x96\n"
      "v_bitop3_b32 v248, v70, v71, v248 bitop3:0x96\n"
      "v_cmp_ne_u32 vcc, s60, v248\n"
      "s_cbranch_vccnz .Lfa_m1\n"
      ".Lfa_m1_ok:\n"
      "v_mfma_f32_16x16x32_bf16 v[192:195], v[242:245], v[60:63], v[192:195]\n"
      "v_mfma_f32_16x16x32_bf16 v[200:203], v[242:245], v[64:67], v[200:203]\n"
      "v_mfma_f32_16x16x32_bf16 v[208:211], v[242:245], v[68:71], v[208:211]\n"
      "v_mfma_f32_16x16x32_bf16 v[196:199], v[240:243], v[60:63], v[196:199]\n"
      "v_mfma_f32_16x16x32_bf16 v[204:207], v[240:243], v[64:67], v[204:207]\n"
      "v_mfma_f32_16x16x32_bf16 v[212:215], v[240:243], v[68:71], v[212:215]\n"
      "v_bitop3_b32 v248, v84, v85, v227 bitop3:0x96\n"
      "v_bitop3_b32 v248, v86, v87, v248 bitop3:0x96\n"
      "v_bitop3_b32 v248, v88, v89, v248 bitop3:0x96\n"
      "v_bitop3_b32 v248, v90, v91, v248 bitop3:0x96\n"
      "v_bitop3_b32 v248, v92, v93, v248 bitop3:0x96\n"
      "v_bitop3_b32 v248, v94, v95, v248 bitop3:0x96\n"
      "v_cmp_ne_u32 vcc, s61, v248\n"
      "s_cbranch_vccnz .Lfa_m2\n"
      ".Lfa_m2_ok:\n"
      "v_mfma_f32_16x16x32_bf16 v[192:195], v[242:245], v[84:87], v[192:195]\n"
      "v_mfma_f32_16x16x32_bf16 v[200:203], v[242:245], v[88:91], v[200:203]\n"
      "v_mfma_f32_16x16x32_bf16 v[208:211], v[242:245], v[92:95], v[208:211]\n"
      "v_mfma_f32_16x16x32_bf16 v[196:199], v[240:243], v[84:87], v[196:199]\n"
      "v_mfma_f32_16x16x32_bf16 v[204:207], v[240:243], v[88:91], v[204:207]\n"
      "v_mfma_f32_16x16x32_bf16 v[212:215], v[240:243], v[92:95], v[212:215]\n"
      "v_bitop3_b32 v248, v108, v109, v228 bitop3:0x96\n"
      "v_bitop3_b32 v248, v110, v111, v248 bitop3:0x96\n"
      "v_bitop3_b32 v248, v112, v113, v248 bitop3:0x96\n"
      "v_bitop3_b32 v248, v114, v115, v248 bitop3:0x96\n"
      "v_bitop3_b32 v248, v116, v117, v248 bitop3:0x96\n"
      "v_bitop3_b32 v248, v118, v119, v248 bitop3:0x96\n"
      "v_cmp_ne_u32 vcc, s62, v248\n"
      "s_cbranch_vccnz .Lfa_m3\n"
      ".Lfa_m3_ok:\n"
      "v_mfma_f32_16x16x32_bf16 v[192:195], v[242:245], v[108:111], v[192:195]\n"
      "v_mfma_f32_16x16x32_bf16 v[200:203], v[242:245], v[112:115], v[200:203]\n"
      "v_mfma_f32_16x16x32_bf16 v[208:211], v[242:245], v[116:119], v[208:211]\n"
      "v_mfma_f32_16x16x32_bf16 v[196:199], v[240:243], v[108:111], v[196:199]\n"
      "v_mfma_f32_16x16x32_bf16 v[204:207], v[240:243], v[112:115], v[204:207]\n"
      "v_mfma_f32_16x16x32_bf16 v[212:215], v[240:243], v[116:119], v[212:215]\n"
      "s_cmp_eq_u32 s21, 25559040\n"
      "s_cbranch_scc1 .Lfa_h4\n"
      "s_waitcnt vmcnt(9)\n"
      "v_bitop3_b32 v248, v132, v133, v229 bitop3:0x96\n"
      "v_bitop3_b32 v248, v134, v135, v248 bitop3:0x96\n"
      "v_bitop3_b32 v248, v136, v137, v248 bitop3:0x96\n"
      "v_bitop3_b32 v248, v138, v139, v248 bitop3:0x96\n"
      "v_bitop3_b32 v248, v140, v141, v248 bitop3:0x96\n"
      "v_bitop3_b32 v248, v142, v143, v248 bitop3:0x96\n"
      "v_cmp_ne_u32 vcc, s63, v248\n"
      "s_cbranch_vccnz .Lfa_h4\n"
      ".Lfa_h4_ok:\n"
      "v_mfma_f32_16x16x32_bf16 v[192:195], v[242:245], v[132:135], v[192:195]\n"
      "v_mfma_f32_16x16x32_bf16 v[200:203], v[242:245], v[136:139], v[200:203]\n"
      "v_mfma_f32_16x16x32_bf16 v[208:211], v[242:245], v[140:143], v[208:211]\n"
      "v_mfma_f32_16x16x32_bf16 v[196:199], v[240:243], v[132:135], v[196:199]\n"
      "v_mfma_f32_16x16x32_bf16 v[204:207], v[240:243], v[136:139], v[204:207]\n"
      "v_mfma_f32_16x16x32_bf16 v[212:215], v[240:243], v[140:143], v[212:215]\n"
      "s_cmp_eq_u32 s44, 25559040\n"
      "s_cbranch_scc1 .Lfa_h5\n"
      "s_waitcnt vmcnt(5)\n"
      "v_bitop3_b32 v248, v156, v157, v230 bitop3:0x96\n"
      "v_bitop3_b32 v248, v158, v159, v248 bitop3:0x96\n"
      "v_bitop3_b32 v248, v160, v161, v248 bitop3:0x96\n"
      "v_bitop3_b32 v248, v162, v163, v248 bitop3:0x96\n"
      "v_bitop3_b32 v248, v164, v165, v248 bitop3:0x96\n"
      "v_bitop3_b32 v248, v166, v167, v248 bitop3:0x96\n"
      "v_cmp_ne_u32 vcc, s64, v248\n"
      "s_cbranch_vccnz .Lfa_h5\n"
      ".Lfa_h5_ok:\n"
      "v_mfma_f32_16x16x32_bf16 v[192:195], v[242:245], v[156:159], v[192:195]\n"
      "v_mfma_f32_16x16x32_bf16 v[200:203], v[242:245], v[160:163], v[200:203]\n"
      "v_mfma_f32_16x16x32_bf16 v[208:211], v[242:245], v[164:167], v[208:211]\n"
      "v_mfma_f32_16x16x32_bf16 v[196:199], v[240:243], v[156:159], v[196:199]\n"
      "v_mfma_f32_16x16x32_bf16 v[204:207], v[240:243], v[160:163], v[204:207]\n"
      "v_mfma_f32_16x16x32_bf16 v[212:215], v[240:243], v[164:167], v[212:215]\n"
      "s_cmp_eq_u32 s45, 25559040\n"
      "s_cbranch_scc1 .Lfa_h6\n"
      "s_waitcnt vmcnt(1)\n"
      "v_bitop3_b32 v248, v180, v181, v246 bitop3:0x96\n"
      "v_bitop3_b32 v248, v182, v183, v248 bitop3:0x96\n"
      "v_bitop3_b32 v248, v184, v185, v248 bitop3:0x96\n"
      "v_bitop3_b32 v248, v186, v187, v248 bitop3:0x96\n"
      "v_bitop3_b32 v248, v188, v189, v248 bitop3:0x96\n"
      "v_bitop3_b32 v248, v190, v191, v248 bitop3:0x96\n"
      "v_cmp_ne_u32 vcc, s65, v248\n"
      "s_cbranch_vccnz .Lfa_h6\n"
      ".Lfa_h6_ok:\n"
      "v_mfma_f32_16x16x32_bf16 v[192:195], v[242:245], v[180:183], v[192:195]\n"
      "v_mfma_f32_16x16x32_bf16 v[200:203], v[242:245], v[184:187], v[200:203]\n"
      "v_mfma_f32_16x16x32_bf16 v[208:211], v[242:245], v[188:191], v[208:211]\n"
      "v_mfma_f32_16x16x32_bf16 v[196:199], v[240:243], v[180:183], v[196:199]\n"
      "v_mfma_f32_16x16x32_bf16 v[204:207], v[240:243], v[184:187], v[204:207]\n"
      "v_mfma_f32_16x16x32_bf16 v[212:215], v[240:243], v[188:191], v[212:215]\n"
      "s_nop 2\n"
      "v_cvt_pk_bf16_f32 v12, v192, v193\n"
      "v_cvt_pk_bf16_f32 v13, v194, v195\n"
      "s_nop 0\n"
      "v_cvt_pk_bf16_f32 v14, v196, v197\n"
      "v_cvt_pk_bf16_f32 v15, v198, v199\n"
      "v_cvt_pk_bf16_f32 v16, v200, v201\n"
      "v_cvt_pk_bf16_f32 v17, v202, v203\n"
      "v_cvt_pk_bf16_f32 v18, v204, v205\n"
      "v_cvt_pk_bf16_f32 v19, v206, v207\n"
      "v_cvt_pk_bf16_f32 v20, v208, v209\n"
      "v_cvt_pk_bf16_f32 v21, v210, v211\n"
      "v_cvt_pk_bf16_f32 v22, v212, v213\n"
      "v_cvt_pk_bf16_f32 v23, v214, v215\n"
      "s_mov_b32 s74, 28672\n"
      "s_mov_b32 s75, 57344\n"
      "v_permlane16_swap_b32 v12, v14\n"
      "v_permlane16_swap_b32 v13, v15\n"
      "buffer_store_dwordx4 v[12:15], v222, s[40:43], 0 offen sc1\n"
      "v_permlane16_swap_b32 v16, v18\n"
      "v_permlane16_swap_b32 v17, v19\n"
      "buffer_store_dwordx4 v[16:19], v222, s[40:43], s74 offen sc1\n"
      "v_permlane16_swap_b32 v20, v22\n"
      "v_permlane16_swap_b32 v21, v23\n"
      "buffer_store_dwordx4 v[20:23], v222, s[40:43], s75 offen sc1\n"
      ".Lfa_end:\n"
      "s_endpgm\n"
      ".Lfa_last:\n"
      "s_nop 7\n"
      "v_cvt_pk_bf16_f32 v20, v208, v209\n"
      "v_cvt_pk_bf16_f32 v21, v210, v211\n"
      "v_bitop3_b32 v247, v20, v21, v247 bitop3:0x96\n"
      "v_cvt_pk_bf16_f32 v22, v212, v213\n"
      "v_cvt_pk_bf16_f32 v23, v214, v215\n"
      "v_bitop3_b32 v247, v22, v23, v247 bitop3:0x96\n"
      "buffer_store_dwordx4 v[12:15], v253, s[24:27], s68 offen offset:0 sc1\n"
      "buffer_store_dwordx4 v[16:19], v253, s[24:27], s68 offen offset:1024 sc1\n"
      "buffer_store_dwordx4 v[20:23], v253, s[24:27], s68 offen offset:2048 sc1\n"
      "buffer_store_dword v247, v221, s[24:27], s68 offen offset:3072 sc1\n"
      "v_mov_b32 v252, s37\n"
      "buffer_store_dword v252, off, s[24:27], s36 offset:0 sc1\n"
      "s_add_u32 s37, s37, 1\n"
      "s_branch .Lfa_end\n"
      ".Lfa_e:\n"
      "s_mov_b32 s67, 0x40000\n"
      ".Lfa_e_l:\n"
      "v_mov_b32 v251, 0\n"
      "s_nop 0\n"
      "s_mov_b64 exec, 0xff\n"
      "buffer_load_dword v251, v220, s[24:27], 0 offen sc1\n"
      "s_mov_b64 exec, -1\n"
      "s_waitcnt vmcnt(0)\n"
      "v_sub_u32 v251, v251, v254\n"
      "v_cmp_lt_u32 vcc, 6, v251\n"
      "s_sub_u32 s67, s67, 1\n"
      "s_cbranch_scc1 .Lfa_e_ok\n"
      "s_nop 3\n"
      "s_and_b32 s74, vcc_lo, 0xff\n"
      "s_cbranch_scc1 .Lfa_e_l\n"
      "s_branch .Lfa_e_ok\n"
      ".Lfa_m0:\n"
      "s_cmp_eq_u32 s46, 7\n"
      "s_cbranch_scc1 .Lfa_m0_ok\n"
      "s_mov_b32 s67, 0x40000\n"
      ".Lfa_m0_l:\n"
      "buffer_load_dwordx4 v[36:39], v253, s[24:27], s52 offen offset:0 sc1\n"
      "buffer_load_dwordx4 v[40:43], v253, s[24:27], s52 offen offset:1024 sc1\n"
      "buffer_load_dwordx4 v[44:47], v253, s[24:27], s52 offen offset:2048 sc1\n"
      "buffer_load_dword v225, v221, s[24:27], s52 offen offset:3072 sc1\n"
      "s_waitcnt vmcnt(0)\n"
      "v_bitop3_b32 v248, v36, v37, v225 bitop3:0x96\n"
      "v_bitop3_b32 v248, v38, v39, v248 bitop3:0x96\n"
      "v_bitop3_b32 v248, v40, v41, v248 bitop3:0x96\n"
      "v_bitop3_b32 v248, v42, v43, v248 bitop3:0x96\n"
      "v_bitop3_b32 v248, v44, v45, v248 bitop3:0x96\n"
      "v_bitop3_b32 v248, v46, v47, v248 bitop3:0x96\n"
      "v_cmp_ne_u32 vcc, s59, v248\n"
      "s_sub_u32 s67, s67, 1\n"
      "s_cbranch_scc1 .Lfa_m0_ok\n"
      "s_cbranch_vccnz .Lfa_m0_l\n"
      "s_branch .Lfa_m0_ok\n"
      ".Lfa_m1:\n"
      "s_cmp_eq_u32 s46, 7\n"
      "s_cbranch_scc1 .Lfa_m1_ok\n"
      "s_mov_b32 s67, 0x40000\n"
      ".Lfa_m1_l:\n"
      "buffer_load_dwordx4 v[60:63], v253, s[24:27], s53 offen offset:0 sc1\n"
      "buffer_load_dwordx4 v[64:67], v253, s[24:27], s53 offen offset:1024 sc1\n"
      "buffer_load_dwordx4 v[68:71], v253, s[24:27], s53 offen offset:2048 sc1\n"
      "buffer_load_dword v226, v221, s[24:27], s53 offen offset:3072 sc1\n"
      "s_waitcnt vmcnt(0)\n"
      "v_bitop3_b32 v248, v60, v61, v226 bitop3:0x96\n"
      "v_bitop3_b32 v248, v62, v63, v248 bitop3:0x96\n"
      "v_bitop3_b32 v248, v64, v65, v248 bitop3:0x96\n"
      "v_bitop3_b32 v248, v66, v67, v248 bitop3:0x96\n"
      "v_bitop3_b32 v248, v68, v69, v248 bitop3:0x96\n"
      "v_bitop3_b32 v248, v70, v71, v248 bitop3:0x96\n"
      "v_cmp_ne_u32 vcc, s60, v248\n"
      "s_sub_u32 s67, s67, 1\n"
      "s_cbranch_scc1 .Lfa_m1_ok\n"
      "s_cbranch_vccnz .Lfa_m1_l\n"
      "s_branch .Lfa_m1_ok\n"
      ".Lfa_m2:\n"
      "s_cmp_eq_u32 s46, 7\n"
      "s_cbranch_scc1 .Lfa_m2_ok\n"
      "s_mov_b32 s67, 0x40000\n"
      ".Lfa_m2_l:\n"
      "buffer_load_dwordx4 v[84:87], v253, s[24:27], s54 offen offset:0 sc1\n"
      "buffer_load_dwordx4 v[88:91], v253, s[24:27], s54 offen offset:1024 sc1\n"
      "buffer_load_dwordx4 v[92:95], v253, s[24:27], s54 offen offset:2048 sc1\n"
      "buffer_load_dword v227, v221, s[24:27], s54 offen offset:3072 sc1\n"
      "s_waitcnt vmcnt(0)\n"
      "v_bitop3_b32 v248, v84, v85, v227 bitop3:0x96\n"
      "v_bitop3_b32 v248, v86, v87, v248 bitop3:0x96\n"
      "v_bitop3_b32 v248, v88, v89, v248 bitop3:0x96\n"
      "v_bitop3_b32 v248, v90, v91, v248 bitop3:0x96\n"
      "v_bitop3_b32 v248, v92, v93, v248 bitop3:0x96\n"
      "v_bitop3_b32 v248, v94, v95, v248 bitop3:0x96\n"
      "v_cmp_ne_u32 vcc, s61, v248\n"
      "s_sub_u32 s67, s67, 1\n"
      "s_cbranch_scc1 .Lfa_m2_ok\n"
      "s_cbranch_vccnz .Lfa_m2_l\n"
      "s_branch .Lfa_m2_ok\n"
      ".Lfa_m3:\n"
      "s_cmp_eq_u32 s46, 7\n"
      "s_cbranch_scc1 .Lfa_m3_ok\n"
      "s_mov_b32 s67, 0x40000\n"
      ".Lfa_m3_l:\n"
      "buffer_load_dwordx4 v[108:111], v253, s[24:27], s55 offen offset:0 sc1\n"
      "buffer_load_dwordx4 v[112:115], v253, s[24:27], s55 offen offset:1024 sc1\n"
      "buffer_load_dwordx4 v[116:119], v253, s[24:27], s55 offen offset:2048 sc1\n"
      "buffer_load_dword v228, v221, s[24:27], s55 offen offset:3072 sc1\n"
      "s_waitcnt vmcnt(0)\n"
      "v_bitop3_b32 v248, v108, v109, v228 bitop3:0x96\n"
      "v_bitop3_b32 v248, v110, v111, v248 bitop3:0x96\n"
      "v_bitop3_b32 v248, v112, v113, v248 bitop3:0x96\n"
      "v_bitop3_b32 v248, v114, v115, v248 bitop3:0x96\n"
      "v_bitop3_b32 v248, v116, v117, v248 bitop3:0x96\n"
      "v_bitop3_b32 v248, v118, v119, v248 bitop3:0x96\n"
      "v_cmp_ne_u32 vcc, s62, v248\n"
      "s_sub_u32 s67, s67, 1\n"
      "s_cbranch_scc1 .Lfa_m3_ok\n"
      "s_cbranch_vccnz .Lfa_m3_l\n"
      "s_branch .Lfa_m3_ok\n"
      ".Lfa_h4:\n"
      "s_mov_b32 s67, 0x40000\n"
      ".Lfa_h4_l:\n"
      "v_mov_b32 v251, 0\n"
      "s_nop 0\n"
      "s_mov_b64 exec, 0xff\n"
      "buffer_load_dword v251, v220, s[24:27], 0 offen sc1\n"
      "s_mov_b64 exec, -1\n"
      "s_waitcnt vmcnt(0)\n"
      "v_readlane_b32 s74, v251, 4\n"
      "s_nop 3\n"
      "s_sub_u32 s74, s74, s19\n"
      "s_sub_u32 s67, s67, 1\n"
      "s_cbranch_scc1 .Lfa_h4_ok\n"
      "s_cmp_le_u32 s74, 6\n"
      "s_cbranch_scc0 .Lfa_h4_l\n"
      "buffer_load_dwordx4 v[132:135], v253, s[24:27], s56 offen offset:0 sc1\n"
      "buffer_load_dwordx4 v[136:139], v253, s[24:27], s56 offen offset:1024 sc1\n"
      "buffer_load_dwordx4 v[140:143], v253, s[24:27], s56 offen offset:2048 sc1\n"
      "buffer_load_dword v229, v221, s[24:27], s56 offen offset:3072 sc1\n"
      "s_waitcnt vmcnt(0)\n"
      "v_bitop3_b32 v248, v132, v133, v229 bitop3:0x96\n"
      "v_bitop3_b32 v248, v134, v135, v248 bitop3:0x96\n"
      "v_bitop3_b32 v248, v136, v137, v248 bitop3:0x96\n"
      "v_bitop3_b32 v248, v138, v139, v248 bitop3:0x96\n"
      "v_bitop3_b32 v248, v140, v141, v248 bitop3:0x96\n"
      "v_bitop3_b32 v248, v142, v143, v248 bitop3:0x96\n"
      "v_cmp_ne_u32 vcc, s63, v248\n"
      "s_cbranch_vccnz .Lfa_h4_l\n"
      "s_branch .Lfa_h4_ok\n"
      ".Lfa_h5:\n"
      "s_mov_b32 s67, 0x40000\n"
      ".Lfa_h5_l:\n"
      "v_mov_b32 v251, 0\n"
      "s_nop 0\n"
      "s_mov_b64 exec, 0xff\n"
      "buffer_load_dword v251, v220, s[24:27], 0 offen sc1\n"
      "s_mov_b64 exec, -1\n"
      "s_waitcnt vmcnt(0)\n"
      "v_readlane_b32 s74, v251, 5\n"
      "s_nop 3\n"
      "s_sub_u32 s74, s74, s38\n"
      "s_sub_u32 s67, s67, 1\n"
      "s_cbranch_scc1 .Lfa_h5_ok\n"
      "s_cmp_le_u32 s74, 6\n"
      "s_cbranch_scc0 .Lfa_h5_l\n"
      "buffer_load_dwordx4 v[156:159], v253, s[24:27], s57 offen offset:0 sc1\n"
      "buffer_load_dwordx4 v[160:163], v253, s[24:27], s57 offen offset:1024 sc1\n"
      "buffer_load_dwordx4 v[164:167], v253, s[24:27], s57 offen offset:2048 sc1\n"
      "buffer_load_dword v230, v221, s[24:27], s57 offen offset:3072 sc1\n"
      "s_waitcnt vmcnt(0)\n"
      "v_bitop3_b32 v248, v156, v157, v230 bitop3:0x96\n"
      "v_bitop3_b32 v248, v158, v159, v248 bitop3:0x96\n"
      "v_bitop3_b32 v248, v160, v161, v248 bitop3:0x96\n"
      "v_bitop3_b32 v248, v162, v163, v248 bitop3:0x96\n"
      "v_bitop3_b32 v248, v164, v165, v248 bitop3:0x96\n"
      "v_bitop3_b32 v248, v166, v167, v248 bitop3:0x96\n"
      "v_cmp_ne_u32 vcc, s64, v248\n"
      "s_cbranch_vccnz .Lfa_h5_l\n"
      "s_branch .Lfa_h5_ok\n"
      ".Lfa_h6:\n"
      "s_mov_b32 s67, 0x40000\n"
      ".Lfa_h6_l:\n"
      "v_mov_b32 v251, 0\n"
      "s_nop 0\n"
      "s_mov_b64 exec, 0xff\n"
      "buffer_load_dword v251, v220, s[24:27], 0 offen sc1\n"
      "s_mov_b64 exec, -1\n"
      "s_waitcnt vmcnt(0)\n"
      "v_readlane_b32 s74, v251, 6\n"
      "s_nop 3\n"
      "s_sub_u32 s74, s74, s39\n"
      "s_sub_u32 s67, s67, 1\n"
      "s_cbranch_scc1 .Lfa_h6_ok\n"
      "s_cmp_le_u32 s74, 6\n"
      "s_cbranch_scc0 .Lfa_h6_l\n"
      "buffer_load_dwordx4 v[180:183], v253, s[24:27], s58 offen offset:0 sc1\n"
      "buffer_load_dwordx4 v[184:187], v253, s[24:27], s58 offen offset:1024 sc1\n"
      "buffer_load_dwordx4 v[188:191], v253, s[24:27], s58 offen offset:2048 sc1\n"
      "buffer_load_dword v246, v221, s[24:27], s58 offen offset:3072 sc1\n"
      "s_waitcnt vmcnt(0)\n"
      "v_bitop3_b32 v248, v180, v181, v246 bitop3:0x96\n"
      "v_bitop3_b32 v248, v182, v183, v248 bitop3:0x96\n"
      "v_bitop3_b32 v248, v184, v185, v248 bitop3:0x96\n"
      "v_bitop3_b32 v248, v186, v187, v248 bitop3:0x96\n"
      "v_bitop3_b32 v248, v188, v189, v248 bitop3:0x96\n"
      "v_bitop3_b32 v248, v190, v191, v248 bitop3:0x96\n"
      "v_cmp_ne_u32 vcc, s65, v248\n"
      "s_cbranch_vccnz .Lfa_h6_l\n"
      "s_branch .Lfa_h6_ok\n"
      : "+{v0}"(tid) : "{s[0:1]}"(ka), "{s2}"(blockIdx.x), "s"((unsigned)(size_t)lds) : "v1", "v2", "v3", "v4", "v5", "v6", "v7", "v8", "v9", "v10", "v11", "v12", "v13", "v14", "v15", "v16", "v17", "v18", "v19", "v20", "v21", "v22", "v23", "v24", "v25", "v26", "v27", "v28", "v29", "v30", "v31", "v32", "v33", "v34", "v35", "v36", "v37", "v38", "v39", "v40", "v41", "v42", "v43", "v44", "v45", "v46", "v47", "v48", "v49", "v50", "v51", "v52", "v53", "v54", "v55", "v56", "v57", "v58", "v59", "v60", "v61", "v62", "v63", "v64", "v65", "v66", "v67", "v68", "v69", "v70", "v71", "v72", "v73", "v74", "v75", "v76", "v77", "v78", "v79", "v80", "v81", "v82", "v83", "v84", "v85", "v86", "v87", "v88", "v89", "v90", "v91", "v92", "v93", "v94", "v95", "v96", "v97", "v98", "v99", "v100", "v101", "v102", "v103", "v104", "v105", "v106", "v107", "v108", "v109", "v110", "v111", "v112", "v113", "v114", "v115", "v116", "v117", "v118", "v119", "v120", "v121", "v122", "v123", "v124", "v125", "v126", "v127", "v128", "v129", "v130", "v131", "v132", "v133", "v134", "v135", "v136", "v137", "v138", "v139", "v140", "v141", "v142", "v143", "v144", "v145", "v146", "v147", "v148", "v149", "v150", "v151", "v152", "v153", "v154", "v155", "v156", "v157", "v158", "v159", "v160", "v161", "v162", "v163", "v164", "v165", "v166", "v167", "v168", "v169", "v170", "v171", "v172", "v173", "v174", "v175", "v176", "v177", "v178", "v179", "v180", "v181", "v182", "v183", "v184", "v185", "v186", "v187", "v188", "v189", "v190", "v191", "v192", "v193", "v194", "v195", "v196", "v197", "v198", "v199", "v200", "v201", "v202", "v203", "v204", "v205", "v206", "v207", "v208", "v209", "v210", "v211", "v212", "v213", "v214", "v215", "v216", "v217", "v218", "v219", "v220", "v221", "v222", "v223", "v224", "v225", "v226", "v227", "v228", "v229", "v230", "v231", "v232", "v233", "v234", "v235", "v236", "v237", "v238", "v239", "v240", "v241", "v242", "v243", "v244", "v245", "v246", "v247", "v248", "v249", "v250", "v251", "v252", "v253", "v254", "v255", "a0", "a1", "a2", "a3", "a4", "a5", "a6", "a7", "a8", "a9", "a10", "a11", "a12", "a13", "a14", "a15", "a16", "a17", "a18", "a19", "a20", "a21", "a22", "a23", "a24", "a25", "a26", "a27", "a28", "a29", "a30", "a31", "a32", "a33", "a34", "a35", "a36", "a37", "a38", "a39", "a40", "a41", "a42", "a43", "a44", "a45", "a46", "a47", "a48", "a49", "a50", "a51", "a52", "a53", "a54", "a55", "a56", "a57", "a58", "a59", "a60", "a61", "a62", "a63", "a64", "a65", "a66", "a67", "a68", "a69", "a70", "a71", "a72", "a73", "a74", "a75", "a76", "a77", "a78", "a79", "a80", "a81", "a82", "a83", "a84", "a85", "a86", "a87", "a88", "a89", "a90", "a91", "a92", "a93", "a94", "a95", "a96", "a97", "a98", "a99", "a100", "a101", "a102", "a103", "a104", "a105", "a106", "a107", "a108", "a109", "a110", "a111", "a112", "a113", "a114", "a115", "a116", "a117", "a118", "a119", "a120", "a121", "a122", "a123", "a124", "a125", "a126", "a127", "a128", "a129", "a130", "a131", "a132", "a133", "a134", "a135", "a136", "a137", "a138", "a139", "a140", "a141", "a142", "a143", "a144", "a145", "a146", "a147", "a148", "a149", "a150", "a151", "a152", "a153", "a154", "a155", "a156", "a157", "a158", "a159", "a160", "a161", "a162", "a163", "a164", "a165", "a166", "a167", "a168", "a169", "a170", "a171", "a172", "a173", "a174", "a175", "a176", "a177", "a178", "a179", "a180", "a181", "a182", "a183", "a184", "a185", "a186", "a187", "a188", "a189", "a190", "a191", "a192", "a193", "a194", "a195", "a196", "a197", "a198", "a199", "a200", "a201", "a202", "a203", "a204", "a205", "a206", "a207", "a208", "a209", "a210", "a211", "a212", "a213", "a214", "a215", "a216", "a217", "a218", "a219", "a220", "a221", "a222", "a223", "a224", "a225", "a226", "a227", "a228", "a229", "a230", "a231", "a232", "a233", "a234", "a235", "a236", "a237", "a238", "a239", "a240", "a241", "a242", "a243", "a244", "a245", "a246", "a247", "a248", "a249", "a250", "a251", "a252", "a253", "a254", "a255", "s3", "s4", "s5", "s6", "s7", "s8", "s9", "s10", "s11", "s12", "s13", "s14", "s15", "s16", "s17", "s18", "s19", "s20", "s21", "s22", "s23", "s24", "s25", "s26", "s27", "s28", "s29", "s30", "s31", "s34", "s35", "s36", "s37", "s38", "s39", "s40", "s41", "s42", "s43", "s44", "s45", "s46", "s47", "s48", "s49", "s50", "s51", "s52", "s53", "s54", "s55", "s56", "s57", "s58", "s59", "s60", "s61", "s62", "s63", "s64", "s65", "s66", "s67", "s68", "s69", "s70", "s71", "s72", "s73", "s74", "s75", "s76", "s77", "s78", "s79", "s80", "s81", "s82", "s83", "s84", "s85", "s86", "s87", "s88", "s89", "s90", "s91", "s92", "s93", "s94", "s95", "s96", "s97", "s98", "s99", "vcc", "scc", "memory");
}

extern "C" void kernel_launch(void* const* d_in, const int* in_sizes, int n_in, void* d_out, int out_size, void* d_ws, size_t ws_size,
                              hipStream_t stream) {
  fused_attn<<<256, 256, 0, stream>>>(d_in[0], d_in[1], d_in[2], d_in[3], d_in[4], d_ws, d_out, d_ws,
                                      d_in[5], d_in[6], d_in[7], d_in[8], d_in[9], d_ws, d_out, d_ws);
}
```
